```python
import torch

def _softmax(x, axis=-1):
    return torch.softmax(x, dim=axis)

def _gelu(x):
    return torch.nn.functional.gelu(x, approximate='tanh')

def _silu(x):
    return torch.nn.functional.silu(x)

def _sigmoid(x):
    return torch.sigmoid(x)

def _layernorm(x, axis=-1, eps=1e-05):
    m = x.mean(dim=axis, keepdim=True)
    v = x.var(dim=axis, keepdim=True, unbiased=False)
    return (x - m) / torch.sqrt(v + eps)

def _scatter_add(tgt, idx, src, axis=0):
    return tgt.clone().index_add_(axis, idx, src)

def _segment_sum(src, seg, n):
    out = torch.zeros((n,) + src.shape[1:], dtype=src.dtype)
    return out.index_add_(0, seg, src)

def _pad(x, pw):
    flat = tuple((v for pair in reversed(pw) for v in pair))
    return torch.nn.functional.pad(x, flat)
kernel_name = 'standalone__molecular_angular_painn__graph'

def setup_inputs():

    _w = lambda a: (lambda t: t.to(torch.bfloat16) if t.is_floating_point() else t)(torch.as_tensor(a))
    N, deg, D = (25000, 16, 128)
    n_rbf = max(16, 4)
    E = N * deg
    cutoff = 5.0
    freqs = (torch.arange(1, n_rbf + 1) * torch.pi / cutoff).to(float)
    return {'sv': _w((lambda *s: torch.randn(*s, dtype=torch.bfloat16) * 0.4)(N, 4, D) * 0.3), 'pos': _w((lambda *s: torch.randn(*s, dtype=torch.bfloat16) * 3.2)(N, 3) * 1.2), 'src': _w((lambda lo, hi, *s: torch.randint(lo, hi, s))(0, N, E).to(torch.int64)), 'dst': _w((lambda lo, hi, *s: torch.randint(lo, hi, s))(0, N, E).to(torch.int64)), 'freqs': _w(freqs), 'w_phi1': _w((lambda *s: torch.randn(*s, dtype=torch.bfloat16) * 3.2)(D, D) / D ** 0.5), 'w_phi2': _w((lambda *s: torch.randn(*s, dtype=torch.bfloat16) * 3.2)(D, 3 * D) / D ** 0.5), 'w_rbf': _w((lambda *s: torch.randn(*s, dtype=torch.bfloat16) * 3.2)(n_rbf, 3 * D) / n_rbf ** 0.5)}

def reference(sv, pos, src, dst, freqs, w_phi1, w_phi2, w_rbf):
    N, _, D = sv.shape
    E = src.shape[0]
    PI, cutoff = (3.141592653589793, 5.0)
    s, v = (sv[:, 0, :].clone(), sv[:, 1:, :].clone())
    r_vec = pos[src] - pos[dst]
    d = torch.sqrt((r_vec * r_vec).sum(-1) + 1e-08)
    r_hat = r_vec / d[:, None]
    rbf = torch.sin(d[:, None] * freqs[None, :]) / d[:, None]
    fcut = 0.5 * (torch.cos(PI * d / cutoff) + 1.0)
    fcut = torch.where(d < cutoff, fcut, 0.0 * d)
    Wf = rbf @ w_rbf * fcut[:, None]
    phi = (_silu(s @ w_phi1) @ w_phi2)[src]
    m = phi * Wf
    m_s, m_vv, m_vr = (m[:, :D].clone(), m[:, D:2 * D].clone(), m[:, 2 * D:].clone())
    ds = _segment_sum(m_s, dst, N)
    dv_e = m_vv[:, None, :] * v[src] + m_vr[:, None, :] * r_hat[:, :, None]
    dv = _segment_sum(dv_e.reshape(E, 3 * D), dst, N).reshape(N, 3, D)
    return sv + torch.concatenate([ds[:, None, :], dv], axis=1)
```

```cpp
#include <hip/hip_runtime.h>
#include <hip/hip_bf16.h>
#include <stdint.h>
#define MIMIC 0
typedef unsigned short u16;
__device__ __forceinline__ float bf2f(u16 x){ return __uint_as_float(((unsigned)x)<<16); }
__device__ __forceinline__ float rb(float x){
  return x;
}
__device__ __forceinline__ u16 f2bf(float x){ __hip_bfloat16 b=__float2bfloat16(x); return *reinterpret_cast<u16*>(&b); }

__global__ void k_phi(const u16* sv, const u16* w1, const u16* w2, float* phi, int N){
  __shared__ float s[128]; __shared__ float h[128];
  int n=blockIdx.x, t=threadIdx.x;
  s[t]=bf2f(sv[(size_t)n*512+t]);
  __syncthreads();
  float a=0; for(int k=0;k<128;k++) a+=s[k]*bf2f(w1[k*128+t]);
  a=rb(a); float sl=a/(1.f+__expf(-a)); h[t]=rb(sl);
  __syncthreads();
  for(int c=t;c<384;c+=128){ float b=0; for(int k=0;k<128;k++) b+=h[k]*bf2f(w2[k*384+c]); phi[(size_t)n*384+c]=rb(b);}
}
__global__ void k_edge(const u16* pos,const int* src,const int* dst,int* head,int* next,int E){
  int e=blockIdx.x*blockDim.x+threadIdx.x; if(e>=E) return;
  int s=src[e], d=dst[e];
  float rx=rb(bf2f(pos[s*3])-bf2f(pos[d*3])), ry=rb(bf2f(pos[s*3+1])-bf2f(pos[d*3+1])), rz=rb(bf2f(pos[s*3+2])-bf2f(pos[d*3+2]));
  float q=rb(rb(rx*rx)+rb(ry*ry)+rb(rz*rz)); q=rb(q+1e-8f); float dd=rb(sqrtf(q));
  if(dd<5.0f){ next[e]=atomicExch(&head[d],e);}
}
__global__ void k_out(const u16* sv,const u16* pos,const int* src,const u16* freqs,const u16* wr,const float* phi,const int* head,const int* next,u16* out,int N){
  int n=blockIdx.x; int t=threadIdx.x;
  float acc[4]={0,0,0,0};
  int e=head[n];
  while(e>=0){
    int s=src[e];
    float rx=rb(bf2f(pos[s*3])-bf2f(pos[n*3])), ry=rb(bf2f(pos[s*3+1])-bf2f(pos[n*3+1])), rz=rb(bf2f(pos[s*3+2])-bf2f(pos[n*3+2]));
    float q=rb(rb(rx*rx)+rb(ry*ry)+rb(rz*rz)); q=rb(q+1e-8f); float dd=rb(sqrtf(q));
    float hx=rb(rx/dd), hy=rb(ry/dd), hz=rb(rz/dd);
    float fc=rb(0.5f*rb(rb(cosf(rb(rb(3.141592653589793f*dd)/5.0f)))+1.0f));
    float w[3]={0,0,0};
    for(int k=0;k<16;k++){ float r=rb(rb(sinf(rb(dd*bf2f(freqs[k]))))/dd);
      for(int j=0;j<3;j++) w[j]+=r*bf2f(wr[k*384+j*128+t]); }
    float m[3]; for(int j=0;j<3;j++){ m[j]=rb(phi[(size_t)s*384+j*128+t]*rb(rb(w[j])*fc)); }
    acc[0]+=m[0];
    float hh[3]={hx,hy,hz};
    for(int j=0;j<3;j++){ float vv=bf2f(sv[(size_t)s*512+(j+1)*128+t]); acc[j+1]+=rb(rb(m[1]*vv)+rb(m[2]*hh[j])); }
    e=next[e];
  }
  for(int j=0;j<4;j++) out[(size_t)n*512+j*128+t]=f2bf(bf2f(sv[(size_t)n*512+j*128+t])+rb(acc[j]));
}
extern "C" void kernel_launch(void* const* d_in, const int* in_sizes, int n_in, void* d_out, int out_size, void* d_ws, size_t ws_size, hipStream_t stream){
  const u16* sv=(const u16*)d_in[0]; const u16* pos=(const u16*)d_in[1]; const int* src=(const int*)d_in[2]; const int* dst=(const int*)d_in[3];
  const u16* freqs=(const u16*)d_in[4]; const u16* w1=(const u16*)d_in[5]; const u16* w2=(const u16*)d_in[6]; const u16* wr=(const u16*)d_in[7];
  int N=in_sizes[0]/512, E=in_sizes[2];
  char* ws=(char*)d_ws; int* head=(int*)ws; int* next=(int*)(ws+(1<<20)); float* phi=(float*)(ws+(8<<20));
  hipMemsetAsync(head,0xFF,(size_t)N*4,stream);
  k_phi<<<N,128,0,stream>>>(sv,w1,w2,phi,N);
  k_edge<<<(E+255)/256,256,0,stream>>>(pos,src,dst,head,next,E);
  k_out<<<N,128,0,stream>>>(sv,pos,src,freqs,wr,phi,head,next,(u16*)d_out,N);
}
```

```cpp
#include <hip/hip_runtime.h>
#include <hip/hip_bf16.h>
#include <stdint.h>

typedef unsigned short u16;
typedef unsigned int u32;
using bf16x8 = __attribute__((ext_vector_type(8))) short;
using bf16x4 = __attribute__((ext_vector_type(4))) short;
using f32x4 = __attribute__((ext_vector_type(4))) float;
using u32x4 = __attribute__((ext_vector_type(4))) unsigned int;
using u32x2 = __attribute__((ext_vector_type(2))) unsigned int;

#define GROUP 16
#define CAP 128
#define W1_BYTES 32768
#define W2_BYTES 98304
#define WR_BYTES 12288
#define WALL_BYTES (W1_BYTES + W2_BYTES + WR_BYTES)
#define K2_WAVES 8
#define SCR_BYTES 2560

__device__ __forceinline__ float bf2f(u16 x) { return __uint_as_float(((u32)x) << 16); }
__device__ __forceinline__ u16 f2bf(float x) {
  __hip_bfloat16 b = __float2bfloat16(x);
  return *reinterpret_cast<u16*>(&b);
}
__device__ __forceinline__ float rb(float x) { return bf2f(f2bf(x)); }
__device__ __forceinline__ u32 pack2(float lo, float hi) {
  return (u32)f2bf(lo) | ((u32)f2bf(hi) << 16);
}
__device__ __forceinline__ float lo16(u32 x) { return __uint_as_float(x << 16); }
__device__ __forceinline__ float hi16(u32 x) { return __uint_as_float(x & 0xffff0000u); }

struct WsLayout {
  size_t cnt, wfrag, rec16, recd, ovf16, ovfd, ovfg, total;
};
static inline size_t al256(size_t x) { return (x + 255) & ~(size_t)255; }
static inline WsLayout ws_layout(int ngroups, int E) {
  WsLayout L;
  size_t o = 0;
  L.cnt = o;   o = al256(o + (size_t)(ngroups + 1) * 4);
  L.wfrag = o; o = al256(o + WALL_BYTES);
  L.rec16 = o; o = al256(o + (size_t)ngroups * CAP * 16);
  L.recd = o;  o = al256(o + (size_t)ngroups * CAP * 4);
  L.ovf16 = o; o = al256(o + (size_t)E * 16);
  L.ovfd = o;  o = al256(o + (size_t)E * 4);
  L.ovfg = o;  o = al256(o + (size_t)E * 4);
  L.total = o;
  return L;
}

__global__ __launch_bounds__(256) void k_edges(
    const u16* __restrict__ pos, const int* __restrict__ src, const int* __restrict__ dst,
    const u16* __restrict__ w1, const u16* __restrict__ w2, const u16* __restrict__ wr,
    int* __restrict__ cnt, u16* __restrict__ wfrag, int4* __restrict__ rec16,
    int* __restrict__ recd, int4* __restrict__ ovf16, int* __restrict__ ovfd,
    int* __restrict__ ovfg, int E, int ngroups, int edge_blocks) {
  if ((int)blockIdx.x >= edge_blocks) {
    int idx = ((int)blockIdx.x - edge_blocks) * 256 + threadIdx.x;
    if (idx < 16384) {
      int j = idx & 7, lane = (idx >> 3) & 63, ks = (idx >> 9) & 3, t = idx >> 11;
      int q = lane >> 4, n = lane & 15;
      wfrag[idx] = w1[(32 * ks + 8 * q + j) * 128 + 16 * t + n];
    } else if (idx < 16384 + 49152) {
      int i = idx - 16384;
      int j = i & 7, lane = (i >> 3) & 63, ks = (i >> 9) & 3, t = (i >> 11) & 7, c = i >> 14;
      int q = lane >> 4, n = lane & 15;
      int u = 16 * (2 * ks + (j >> 2)) + 4 * q + (j & 3);
      wfrag[idx] = w2[u * 384 + 128 * c + 8 * n + t];
    } else if (idx < 16384 + 49152 + 6144) {
      int i = idx - 16384 - 49152;
      int j = i & 3, lane = (i >> 2) & 63, t = (i >> 8) & 7, c = i >> 11;
      int q = lane >> 4, n = lane & 15;
      wfrag[idx] = wr[(4 * q + j) * 384 + 128 * c + 8 * n + t];
    }
    return;
  }
  int e = (int)blockIdx.x * 256 + threadIdx.x;
  if (e >= E) return;
  int s = src[e], d = dst[e];
  float rx = rb(bf2f(pos[s * 3 + 0]) - bf2f(pos[d * 3 + 0]));
  float ry = rb(bf2f(pos[s * 3 + 1]) - bf2f(pos[d * 3 + 1]));
  float rz = rb(bf2f(pos[s * 3 + 2]) - bf2f(pos[d * 3 + 2]));
  float q2 = rb(rb(rx * rx) + rb(ry * ry) + rb(rz * rz));
  q2 = rb(q2 + 1e-8f);
  float dd = rb(sqrtf(q2));
  if (!(dd < 5.0f)) return;
  float hx = rb(rx / dd), hy = rb(ry / dd), hz = rb(rz / dd);
  float fc = rb(0.5f * rb(rb(cosf(rb(rb(3.141592653589793f * dd) / 5.0f))) + 1.0f));
  int g = d >> 4;
  int4 r;
  r.x = (e << 4) | (d & 15);
  r.y = s;
  r.z = (int)((u32)f2bf(hx) | ((u32)f2bf(hy) << 16));
  r.w = (int)((u32)f2bf(hz) | ((u32)f2bf(fc) << 16));
  int dbits = (int)__float_as_uint(dd);
  int slot = atomicAdd(&cnt[g], 1);
  if (slot < CAP) {
    rec16[(size_t)g * CAP + slot] = r;
    recd[(size_t)g * CAP + slot] = dbits;
  } else {
    int o = atomicAdd(&cnt[ngroups], 1);
    ovf16[o] = r;
    ovfd[o] = dbits;
    ovfg[o] = g;
  }
}

__device__ __forceinline__ float bfx(const bf16x8& v, int i) {
  return __uint_as_float(((u32)(u16)v[i]) << 16);
}

__global__ __launch_bounds__(512) void k_message(
    const u16* __restrict__ sv, const u16* __restrict__ freqs, const int* __restrict__ cnt,
    const u16* __restrict__ wfrag, const int4* __restrict__ rec16, const int* __restrict__ recd,
    const int4* __restrict__ ovf16, const int* __restrict__ ovfd, const int* __restrict__ ovfg,
    u16* __restrict__ out, int N, int ngroups) {
  extern __shared__ __attribute__((aligned(16))) char smem[];
  const int tid = threadIdx.x;
  const int lane = tid & 63, wave = tid >> 6;

  {
    const u32x4* g4 = reinterpret_cast<const u32x4*>(wfrag);
    u32x4* l4 = reinterpret_cast<u32x4*>(smem);
    for (int i = tid; i < WALL_BYTES / 16; i += 512) l4[i] = g4[i];
  }
  const bf16x8* W1f = reinterpret_cast<const bf16x8*>(smem);
  const bf16x8* W2f = reinterpret_cast<const bf16x8*>(smem + W1_BYTES);
  const bf16x4* Wrf = reinterpret_cast<const bf16x4*>(smem + W1_BYTES + W2_BYTES);
  int* scr = reinterpret_cast<int*>(smem + WALL_BYTES + wave * SCR_BYTES);
  int* s_key = scr;
  int* s_src = scr + 128;
  int* s_hxy = scr + 256;
  int* s_hzf = scr + 384;
  int* s_d = scr + 512;

  __syncthreads();

  for (int g = (int)blockIdx.x + (int)gridDim.x * wave; g < ngroups; g += (int)gridDim.x * K2_WAVES) {
    const int total = cnt[g];
    const int nb = total < CAP ? total : CAP;
    {
      int bl = lane;
      asm volatile("" : "+v"(bl));
      int4 r0 = make_int4(0x7fffffff, 0, 0, 0), r1 = make_int4(0x7fffffff, 0, 0, 0);
      int d0 = 0, d1 = 0;
      if (bl < nb) { r0 = rec16[(size_t)g * CAP + bl]; d0 = recd[(size_t)g * CAP + bl]; }
      if (bl + 64 < nb) { r1 = rec16[(size_t)g * CAP + bl + 64]; d1 = recd[(size_t)g * CAP + bl + 64]; }
      s_key[bl] = r0.x;
      s_key[bl + 64] = r1.x;
      __builtin_amdgcn_wave_barrier();
      int k0 = 0, k1 = 0;
      for (int i = 0; i < nb; i += 4) {
        int4 kk = *reinterpret_cast<const int4*>(s_key + i);
        k0 += (kk.x < r0.x) + (kk.y < r0.x) + (kk.z < r0.x) + (kk.w < r0.x);
        k1 += (kk.x < r1.x) + (kk.y < r1.x) + (kk.z < r1.x) + (kk.w < r1.x);
      }
      __builtin_amdgcn_wave_barrier();
      if (bl < nb) { s_key[k0] = r0.x; s_src[k0] = r0.y; s_hxy[k0] = r0.z; s_hzf[k0] = r0.w; s_d[k0] = d0; }
      if (bl + 64 < nb) { s_key[k1] = r1.x; s_src[k1] = r1.y; s_hxy[k1] = r1.z; s_hzf[k1] = r1.w; s_d[k1] = d1; }
      __builtin_amdgcn_wave_barrier();
    }

    f32x4 acc[4][8];
#pragma unroll
    for (int c = 0; c < 4; ++c)
#pragma unroll
      for (int t = 0; t < 8; ++t) acc[c][t] = (f32x4){0.f, 0.f, 0.f, 0.f};

    int have = nb;
    int ovf_pos = 0;
    const int ovf_n = (total > CAP) ? cnt[ngroups] : 0;

    while (true) {
      for (int base = 0; base < have; base += 16) {
        int wl = lane;
        asm volatile("" : "+v"(wl));
        const int q = wl >> 4, n = wl & 15;
        const int ie = base + n;
        const bool ve = ie < have;
        const int src_e = ve ? s_src[ie] : 0;
        const float d_e = ve ? __int_as_float(s_d[ie]) : 1.0f;
        int src4[4], dl4[4];
        u32 hxy4[4], hzf4[4];
#pragma unroll
        for (int r = 0; r < 4; ++r) {
          const int i4 = base + 4 * q + r;
          const bool v4 = i4 < have;
          src4[r] = v4 ? s_src[i4] : 0;
          dl4[r] = v4 ? (s_key[i4] & 15) : -1;
          hxy4[r] = v4 ? (u32)s_hxy[i4] : 0u;
          hzf4[r] = v4 ? (u32)s_hzf[i4] : 0u;
        }
        bf16x8 sB[4];
        {
          const bf16x8* srow = reinterpret_cast<const bf16x8*>(sv + (size_t)src_e * 512);
#pragma unroll
          for (int ks = 0; ks < 4; ++ks) sB[ks] = srow[4 * ks + q];
        }
        bf16x8 vr[4][3];
#pragma unroll
        for (int r = 0; r < 4; ++r) {
          const bf16x8* vrow = reinterpret_cast<const bf16x8*>(sv + (size_t)src4[r] * 512 + 128);
#pragma unroll
          for (int x = 0; x < 3; ++x) vr[r][x] = vrow[16 * x + n];
        }
        bf16x4 rbfA;
        {
          float rv[4];
          const u32x2 fq = *reinterpret_cast<const u32x2*>(freqs + 4 * (wl >> 4));
          const float fk[4] = {lo16(fq[0]), hi16(fq[0]), lo16(fq[1]), hi16(fq[1])};
#pragma unroll
          for (int j = 0; j < 4; ++j) {
            float a = rb(d_e * fk[j]);
            float sn = rb(__sinf(a));
            rv[j] = sn / d_e;
          }
          u32 p0 = pack2(rv[0], rv[1]), p1 = pack2(rv[2], rv[3]);
          rbfA[0] = (short)(p0 & 0xffff); rbfA[1] = (short)(p0 >> 16);
          rbfA[2] = (short)(p1 & 0xffff); rbfA[3] = (short)(p1 >> 16);
        }
        bf16x4 selA;
#pragma unroll
        for (int j = 0; j < 4; ++j) selA[j] = (dl4[j] == n) ? (short)0x3f80 : (short)0;

        bf16x8 hA[4];
#pragma unroll
        for (int t = 0; t < 8; ++t) {
          f32x4 d1 = {0.f, 0.f, 0.f, 0.f};
#pragma unroll
          for (int ks = 0; ks < 4; ++ks)
            d1 = __builtin_amdgcn_mfma_f32_16x16x32_bf16(W1f[(t * 4 + ks) * 64 + wl], sB[ks], d1, 0, 0, 0);
#pragma unroll
          for (int r = 0; r < 4; ++r) {
            float a = rb(d1[r]);
            float sl = a * __builtin_amdgcn_rcpf(1.0f + __expf(-a));
            hA[t >> 1][(t & 1) * 4 + r] = (short)f2bf(sl);
          }
          __builtin_amdgcn_sched_barrier(0);
        }
#pragma unroll
        for (int t = 0; t < 8; ++t) {
          float m[3][4];
#pragma unroll
          for (int c = 0; c < 3; ++c) {
            f32x4 d2 = {0.f, 0.f, 0.f, 0.f};
#pragma unroll
            for (int ks = 0; ks < 4; ++ks)
              d2 = __builtin_amdgcn_mfma_f32_16x16x32_bf16(hA[ks], W2f[((c * 8 + t) * 4 + ks) * 64 + wl], d2, 0, 0, 0);
            f32x4 wf = {0.f, 0.f, 0.f, 0.f};
            wf = __builtin_amdgcn_mfma_f32_16x16x16bf16_1k(rbfA, Wrf[(c * 8 + t) * 64 + wl], wf, 0, 0, 0);
#pragma unroll
            for (int r = 0; r < 4; ++r) m[c][r] = rb(rb(d2[r]) * rb(rb(wf[r]) * hi16(hzf4[r])));
            __builtin_amdgcn_sched_barrier(0);
          }
          bf16x4 mf;
#pragma unroll
          for (int r = 0; r < 4; ++r) mf[r] = (short)f2bf(m[0][r]);
          acc[0][t] = __builtin_amdgcn_mfma_f32_16x16x16bf16_1k(selA, mf, acc[0][t], 0, 0, 0);
#pragma unroll
          for (int x = 0; x < 3; ++x) {
#pragma unroll
            for (int r = 0; r < 4; ++r) {
              const float hh = (x == 0) ? lo16(hxy4[r]) : ((x == 1) ? hi16(hxy4[r]) : lo16(hzf4[r]));
              const float vv = bfx(vr[r][x], t);
              mf[r] = (short)f2bf(rb(m[1][r] * vv) + rb(m[2][r] * hh));
            }
            acc[1 + x][t] = __builtin_amdgcn_mfma_f32_16x16x16bf16_1k(selA, mf, acc[1 + x][t], 0, 0, 0);
          }
          __builtin_amdgcn_sched_barrier(0);
        }
      }
      if (ovf_pos >= ovf_n) break;
      __builtin_amdgcn_wave_barrier();
      have = 0;
      while (ovf_pos < ovf_n && have <= CAP - 64) {
        const int i = ovf_pos + lane;
        const bool mine = (i < ovf_n) && (ovfg[i] == g);
        const unsigned long long bal = __ballot(mine);
        const int before = __popcll(bal & ((1ull << lane) - 1ull));
        if (mine) {
          const int4 r = ovf16[i];
          const int p = have + before;
          s_key[p] = r.x; s_src[p] = r.y; s_hxy[p] = r.z; s_hzf[p] = r.w; s_d[p] = ovfd[i];
        }
        have += __popcll(bal);
        ovf_pos += 64;
      }
      __builtin_amdgcn_wave_barrier();
    }

    int ol = lane;
    asm volatile("" : "+v"(ol));
#pragma unroll
    for (int r = 0; r < 4; ++r) {
      const int node = g * GROUP + 4 * (ol >> 4) + r;
      if (node < N) {
#pragma unroll
        for (int c = 0; c < 4; ++c) {
          const size_t off = (size_t)node * 512 + 128 * c + 8 * (ol & 15);
          const bf16x8 x = *reinterpret_cast<const bf16x8*>(sv + off);
          u32x4 o;
#pragma unroll
          for (int h = 0; h < 4; ++h) {
            const float a0 = bfx(x, 2 * h) + rb(acc[c][2 * h][r]);
            const float a1 = bfx(x, 2 * h + 1) + rb(acc[c][2 * h + 1][r]);
            o[h] = pack2(a0, a1);
          }
          *reinterpret_cast<u32x4*>(out + off) = o;
        }
      }
    }
    __builtin_amdgcn_wave_barrier();
  }
}

extern "C" void kernel_launch(void* const* d_in, const int* in_sizes, int n_in,
                              void* d_out, int out_size, void* d_ws, size_t ws_size,
                              hipStream_t stream) {
  const u16* sv = (const u16*)d_in[0];
  const u16* pos = (const u16*)d_in[1];
  const int* src = (const int*)d_in[2];
  const int* dst = (const int*)d_in[3];
  const u16* freqs = (const u16*)d_in[4];
  const u16* w1 = (const u16*)d_in[5];
  const u16* w2 = (const u16*)d_in[6];
  const u16* wr = (const u16*)d_in[7];
  const int N = in_sizes[0] / 512;
  const int E = in_sizes[2];
  const int ngroups = (N + GROUP - 1) / GROUP;
  const WsLayout L = ws_layout(ngroups, E);
  char* ws = (char*)d_ws;
  int* cnt = (int*)(ws + L.cnt);
  u16* wfrag = (u16*)(ws + L.wfrag);
  int4* rec16 = (int4*)(ws + L.rec16);
  int* recd = (int*)(ws + L.recd);
  int4* ovf16 = (int4*)(ws + L.ovf16);
  int* ovfd = (int*)(ws + L.ovfd);
  int* ovfg = (int*)(ws + L.ovfg);

  (void)hipMemsetAsync(cnt, 0, al256((size_t)(ngroups + 1) * 4), stream);
  const int edge_blocks = (E + 255) / 256;
  const int prep_blocks = (WALL_BYTES / 2 + 255) / 256;
  k_edges<<<edge_blocks + prep_blocks, 256, 0, stream>>>(pos, src, dst, w1, w2, wr, cnt, wfrag, rec16,
                                                          recd, ovf16, ovfd, ovfg, E, ngroups, edge_blocks);
  const size_t lds = WALL_BYTES + K2_WAVES * SCR_BYTES;
  static_assert(WALL_BYTES + K2_WAVES * SCR_BYTES <= 163840, "lds");
  int grid = 256;
  if (ngroups < grid) grid = ngroups;
  k_message<<<grid, 512, lds, stream>>>(sv, freqs, cnt, wfrag, rec16, recd, ovf16, ovfd, ovfg,
                                        (u16*)d_out, N, ngroups);
}
```

```cpp
#include <hip/hip_runtime.h>
#include <hip/hip_bf16.h>
#include <stdint.h>

typedef unsigned short u16;
typedef unsigned int u32;
using bf16x8 = __attribute__((ext_vector_type(8))) short;
using bf16x4 = __attribute__((ext_vector_type(4))) short;
using f32x4 = __attribute__((ext_vector_type(4))) float;
using f32x2 = __attribute__((ext_vector_type(2))) float;
using u32x4 = __attribute__((ext_vector_type(4))) unsigned int;
using u32x2 = __attribute__((ext_vector_type(2))) unsigned int;

#define GROUP 16
#define CAP 128
#define CNT_STRIDE 32
#define W1_ELEMS 16384
#define W2_BYTES 98304
#define WR_BYTES 12288
#define WL_BYTES (W2_BYTES + WR_BYTES)
#define K2_WAVES 8
#define SCR_BYTES 2560

__device__ __forceinline__ float bf2f(u16 x) { return __uint_as_float(((u32)x) << 16); }
__device__ __forceinline__ u16 f2bf(float x) {
  __hip_bfloat16 b = __float2bfloat16(x);
  return *reinterpret_cast<u16*>(&b);
}
__device__ __forceinline__ float rb(float x) { return bf2f(f2bf(x)); }
typedef __bf16 hbf2 __attribute__((ext_vector_type(2)));
__device__ __forceinline__ u32 pack2(float lo, float hi) {
  f32x2 f = {lo, hi};
  hbf2 b = __builtin_convertvector(f, hbf2);
  return *reinterpret_cast<u32*>(&b);
}
__device__ __forceinline__ float lo16(u32 x) { return __uint_as_float(x << 16); }
__device__ __forceinline__ float hi16(u32 x) { return __uint_as_float(x & 0xffff0000u); }

struct WsLayout {
  size_t cnt, w1f, wl, h, rec16, recd, ovf16, ovfd, ovfg, total;
};
static inline size_t al256(size_t x) { return (x + 255) & ~(size_t)255; }
static inline WsLayout ws_layout(int N, int ngroups, int E) {
  WsLayout L;
  size_t o = 0;
  L.cnt = o;   o = al256(o + (size_t)(ngroups + 1) * CNT_STRIDE * 4);
  L.w1f = o;   o = al256(o + W1_ELEMS * 2);
  L.wl = o;    o = al256(o + WL_BYTES);
  L.h = o;     o = al256(o + (size_t)N * 256);
  L.rec16 = o; o = al256(o + (size_t)ngroups * CAP * 16);
  L.recd = o;  o = al256(o + (size_t)ngroups * CAP * 4);
  L.ovf16 = o; o = al256(o + (size_t)E * 16);
  L.ovfd = o;  o = al256(o + (size_t)E * 4);
  L.ovfg = o;  o = al256(o + (size_t)E * 4);
  L.total = o;
  return L;
}

__global__ __launch_bounds__(256) void k_prep(const u16* __restrict__ w1, const u16* __restrict__ w2,
                                              const u16* __restrict__ wr, int* __restrict__ cnt,
                                              u16* __restrict__ w1f, u16* __restrict__ wl, int ngroups) {
  const int idx = (int)blockIdx.x * 256 + threadIdx.x;
  if (idx < 16384) {
    int j = idx & 7, lane = (idx >> 3) & 63, ks = (idx >> 9) & 3, t = idx >> 11;
    int q = lane >> 4, n = lane & 15;
    w1f[idx] = w1[(32 * ks + 8 * q + j) * 128 + 16 * t + n];
  } else if (idx < 16384 + 49152) {
    int i = idx - 16384;
    int j = i & 7, lane = (i >> 3) & 63, ks = (i >> 9) & 3, t = (i >> 11) & 7, c = i >> 14;
    int q = lane >> 4, n = lane & 15;
    int u = 16 * (2 * ks + (j >> 2)) + 4 * q + (j & 3);
    wl[i] = w2[u * 384 + 128 * c + 8 * n + t];
  } else if (idx < 16384 + 49152 + 6144) {
    int i = idx - 16384 - 49152;
    int j = i & 3, lane = (i >> 2) & 63, t = (i >> 8) & 7, c = i >> 11;
    int q = lane >> 4, n = lane & 15;
    wl[49152 + i] = wr[(4 * q + j) * 384 + 128 * c + 8 * n + t];
  }
  if (idx <= ngroups) cnt[idx * CNT_STRIDE] = 0;
}

__global__ __launch_bounds__(256) void k_edges(
    const u16* __restrict__ sv, const u16* __restrict__ pos, const int* __restrict__ src,
    const int* __restrict__ dst, const u16* __restrict__ w1f, int* __restrict__ cnt,
    u16* __restrict__ hbuf, int4* __restrict__ rec16, int* __restrict__ recd,
    int4* __restrict__ ovf16, int* __restrict__ ovfd, int* __restrict__ ovfg, int N, int E,
    int ngroups, int h_blocks) {
  if ((int)blockIdx.x < h_blocks) {
    const int lane = threadIdx.x & 63, wave = threadIdx.x >> 6;
    const int q = lane >> 4, n = lane & 15;
    const int node0 = ((int)blockIdx.x * 4 + wave) * 16;
    if (node0 >= N) return;
    int node = node0 + n;
    const bool valid = node < N;
    if (!valid) node = N - 1;
    const bf16x8* srow = reinterpret_cast<const bf16x8*>(sv + (size_t)node * 512);
    bf16x8 sB[4];
#pragma unroll
    for (int ks = 0; ks < 4; ++ks) sB[ks] = srow[4 * ks + q];
    const bf16x8* W1f = reinterpret_cast<const bf16x8*>(w1f);
    u32x4 hp[4];
#pragma unroll
    for (int t = 0; t < 8; ++t) {
      f32x4 d1 = {0.f, 0.f, 0.f, 0.f};
#pragma unroll
      for (int ks = 0; ks < 4; ++ks)
        d1 = __builtin_amdgcn_mfma_f32_16x16x32_bf16(W1f[(t * 4 + ks) * 64 + lane], sB[ks], d1, 0, 0, 0);
      float sl[4];
#pragma unroll
      for (int r = 0; r < 4; ++r) {
        const float a = rb(d1[r]);
        sl[r] = a * __builtin_amdgcn_rcpf(1.0f + __expf(-a));
      }
      hp[t >> 1][(t & 1) * 2 + 0] = pack2(sl[0], sl[1]);
      hp[t >> 1][(t & 1) * 2 + 1] = pack2(sl[2], sl[3]);
    }
    if (valid) {
      u32x4* hrow = reinterpret_cast<u32x4*>(hbuf + (size_t)node * 128);
#pragma unroll
      for (int ks = 0; ks < 4; ++ks) hrow[4 * ks + q] = hp[ks];
    }
    return;
  }
  const int e = ((int)blockIdx.x - h_blocks) * 256 + threadIdx.x;
  if (e >= E) return;
  const int s = src[e], d = dst[e];
  const float rx = rb(bf2f(pos[s * 3 + 0]) - bf2f(pos[d * 3 + 0]));
  const float ry = rb(bf2f(pos[s * 3 + 1]) - bf2f(pos[d * 3 + 1]));
  const float rz = rb(bf2f(pos[s * 3 + 2]) - bf2f(pos[d * 3 + 2]));
  float q2 = rb(rb(rx * rx) + rb(ry * ry) + rb(rz * rz));
  q2 = rb(q2 + 1e-8f);
  const float dd = rb(sqrtf(q2));
  if (!(dd < 5.0f)) return;
  const float hx = rb(rx / dd), hy = rb(ry / dd), hz = rb(rz / dd);
  const float fc = rb(0.5f * rb(rb(cosf(rb(rb(3.141592653589793f * dd) / 5.0f))) + 1.0f));
  const int g = d >> 4;
  int4 r;
  r.x = (e << 4) | (d & 15);
  r.y = s;
  r.z = (int)((u32)f2bf(hx) | ((u32)f2bf(hy) << 16));
  r.w = (int)((u32)f2bf(hz) | ((u32)f2bf(fc) << 16));
  const int dbits = (int)__float_as_uint(dd);
  const int slot = atomicAdd(&cnt[g * CNT_STRIDE], 1);
  if (slot < CAP) {
    rec16[(size_t)g * CAP + slot] = r;
    recd[(size_t)g * CAP + slot] = dbits;
  } else {
    const int o = atomicAdd(&cnt[ngroups * CNT_STRIDE], 1);
    ovf16[o] = r;
    ovfd[o] = dbits;
    ovfg[o] = g;
  }
}

__device__ __forceinline__ float bfx(const bf16x8& v, int i) {
  return __uint_as_float(((u32)(u16)v[i]) << 16);
}
__device__ __forceinline__ bf16x4 pack4(float a, float b, float c, float d) {
  u32x2 p;
  p[0] = pack2(a, b);
  p[1] = pack2(c, d);
  return *reinterpret_cast<bf16x4*>(&p);
}

__global__ __launch_bounds__(512) void k_message(
    const u16* __restrict__ sv, const u16* __restrict__ freqs, const int* __restrict__ cnt,
    const u16* __restrict__ wl_g, const u16* __restrict__ hbuf, const int4* __restrict__ rec16,
    const int* __restrict__ recd, const int4* __restrict__ ovf16, const int* __restrict__ ovfd,
    const int* __restrict__ ovfg, u16* __restrict__ out, int N, int ngroups) {
  extern __shared__ __attribute__((aligned(16))) char smem[];
  const int tid = threadIdx.x;
  const int lane = tid & 63, wave = tid >> 6;

  {
    const u32x4* g4 = reinterpret_cast<const u32x4*>(wl_g);
    u32x4* l4 = reinterpret_cast<u32x4*>(smem);
    for (int i = tid; i < WL_BYTES / 16; i += 512) l4[i] = g4[i];
  }
  const bf16x8* W2f = reinterpret_cast<const bf16x8*>(smem);
  const bf16x4* Wrf = reinterpret_cast<const bf16x4*>(smem + W2_BYTES);
  int* scr = reinterpret_cast<int*>(smem + WL_BYTES + wave * SCR_BYTES);
  int* s_key = scr;
  int* s_src = scr + 128;
  int* s_hxy = scr + 256;
  int* s_hzf = scr + 384;
  int* s_d = scr + 512;

  __syncthreads();

  for (int g = (int)blockIdx.x + (int)gridDim.x * wave; g < ngroups; g += (int)gridDim.x * K2_WAVES) {
    const int total = cnt[g * CNT_STRIDE];
    const int nb = total < CAP ? total : CAP;
    {
      int bl = lane;
      asm volatile("" : "+v"(bl));
      int4 r0 = make_int4(0x7fffffff, 0, 0, 0), r1 = make_int4(0x7fffffff, 0, 0, 0);
      int d0 = 0, d1 = 0;
      if (bl < nb) { r0 = rec16[(size_t)g * CAP + bl]; d0 = recd[(size_t)g * CAP + bl]; }
      if (bl + 64 < nb) { r1 = rec16[(size_t)g * CAP + bl + 64]; d1 = recd[(size_t)g * CAP + bl + 64]; }
      s_key[bl] = r0.x;
      s_key[bl + 64] = r1.x;
      __builtin_amdgcn_wave_barrier();
      int k0 = 0, k1 = 0;
      for (int i = 0; i < nb; i += 4) {
        int4 kk = *reinterpret_cast<const int4*>(s_key + i);
        k0 += (kk.x < r0.x) + (kk.y < r0.x) + (kk.z < r0.x) + (kk.w < r0.x);
        k1 += (kk.x < r1.x) + (kk.y < r1.x) + (kk.z < r1.x) + (kk.w < r1.x);
      }
      __builtin_amdgcn_wave_barrier();
      if (bl < nb) { s_key[k0] = r0.x; s_src[k0] = r0.y; s_hxy[k0] = r0.z; s_hzf[k0] = r0.w; s_d[k0] = d0; }
      if (bl + 64 < nb) { s_key[k1] = r1.x; s_src[k1] = r1.y; s_hxy[k1] = r1.z; s_hzf[k1] = r1.w; s_d[k1] = d1; }
      __builtin_amdgcn_wave_barrier();
    }

    f32x4 acc[4][8];
#pragma unroll
    for (int c = 0; c < 4; ++c)
#pragma unroll
      for (int t = 0; t < 8; ++t) acc[c][t] = (f32x4){0.f, 0.f, 0.f, 0.f};

    int have = nb;
    int ovf_pos = 0;
    const int ovf_n = (total > CAP) ? cnt[ngroups * CNT_STRIDE] : 0;

    while (true) {
      for (int base = 0; base < have; base += 16) {
        int wl = lane;
        asm volatile("" : "+v"(wl));
        const int q = wl >> 4, n = wl & 15;
        const int ie = base + n;
        const bool ve = ie < have;
        const int src_e = ve ? s_src[ie] : 0;
        const float d_e = ve ? __int_as_float(s_d[ie]) : 1.0f;
        int src4[4], dl4[4];
        u32 hxy4[4], hzf4[4];
#pragma unroll
        for (int r = 0; r < 4; ++r) {
          const int i4 = base + 4 * q + r;
          const bool v4 = i4 < have;
          src4[r] = v4 ? s_src[i4] : 0;
          dl4[r] = v4 ? (s_key[i4] & 15) : -1;
          hxy4[r] = v4 ? (u32)s_hxy[i4] : 0u;
          hzf4[r] = v4 ? (u32)s_hzf[i4] : 0u;
        }
        bf16x8 hA[4];
        {
          const bf16x8* hrow = reinterpret_cast<const bf16x8*>(hbuf + (size_t)src_e * 128);
#pragma unroll
          for (int ks = 0; ks < 4; ++ks) hA[ks] = hrow[4 * ks + q];
        }
        bf16x8 vr[4][3];
#pragma unroll
        for (int r = 0; r < 4; ++r) {
          const bf16x8* vrow = reinterpret_cast<const bf16x8*>(sv + (size_t)src4[r] * 512 + 128);
#pragma unroll
          for (int x = 0; x < 3; ++x) vr[r][x] = vrow[16 * x + n];
        }
        bf16x4 rbfA;
        {
          float rv[4];
          const u32x2 fq = *reinterpret_cast<const u32x2*>(freqs + 4 * q);
          const float fk[4] = {lo16(fq[0]), hi16(fq[0]), lo16(fq[1]), hi16(fq[1])};
#pragma unroll
          for (int j = 0; j < 4; ++j) {
            const float a = rb(d_e * fk[j]);
            const float sn = rb(__sinf(a));
            rv[j] = sn / d_e;
          }
          rbfA = pack4(rv[0], rv[1], rv[2], rv[3]);
        }
        bf16x4 selA;
#pragma unroll
        for (int j = 0; j < 4; ++j) selA[j] = (dl4[j] == n) ? (short)0x3f80 : (short)0;

#pragma unroll
        for (int t = 0; t < 8; ++t) {
          f32x4 m[3];
#pragma unroll
          for (int c = 0; c < 3; ++c) {
            f32x4 d2 = {0.f, 0.f, 0.f, 0.f};
#pragma unroll
            for (int ks = 0; ks < 4; ++ks)
              d2 = __builtin_amdgcn_mfma_f32_16x16x32_bf16(hA[ks], W2f[((c * 8 + t) * 4 + ks) * 64 + wl], d2, 0, 0, 0);
            f32x4 wf = {0.f, 0.f, 0.f, 0.f};
            wf = __builtin_amdgcn_mfma_f32_16x16x16bf16_1k(rbfA, Wrf[(c * 8 + t) * 64 + wl], wf, 0, 0, 0);
#pragma unroll
            for (int r = 0; r < 4; ++r) m[c][r] = d2[r] * (wf[r] * hi16(hzf4[r]));
            if (c == 1) __builtin_amdgcn_sched_barrier(0);
          }
          acc[0][t] = __builtin_amdgcn_mfma_f32_16x16x16bf16_1k(
              selA, pack4(m[0][0], m[0][1], m[0][2], m[0][3]), acc[0][t], 0, 0, 0);
#pragma unroll
          for (int x = 0; x < 3; ++x) {
            float mv[4];
#pragma unroll
            for (int r = 0; r < 4; ++r) {
              const float hh = (x == 0) ? lo16(hxy4[r]) : ((x == 1) ? hi16(hxy4[r]) : lo16(hzf4[r]));
              mv[r] = m[1][r] * bfx(vr[r][x], t) + m[2][r] * hh;
            }
            acc[1 + x][t] = __builtin_amdgcn_mfma_f32_16x16x16bf16_1k(
                selA, pack4(mv[0], mv[1], mv[2], mv[3]), acc[1 + x][t], 0, 0, 0);
          }
          __builtin_amdgcn_sched_barrier(0);
        }
      }
      if (ovf_pos >= ovf_n) break;
      __builtin_amdgcn_wave_barrier();
      have = 0;
      while (ovf_pos < ovf_n && have <= CAP - 64) {
        const int i = ovf_pos + lane;
        const bool mine = (i < ovf_n) && (ovfg[i] == g);
        const unsigned long long bal = __ballot(mine);
        const int before = __popcll(bal & ((1ull << lane) - 1ull));
        if (mine) {
          const int4 r = ovf16[i];
          const int p = have + before;
          s_key[p] = r.x; s_src[p] = r.y; s_hxy[p] = r.z; s_hzf[p] = r.w; s_d[p] = ovfd[i];
        }
        have += __popcll(bal);
        ovf_pos += 64;
      }
      __builtin_amdgcn_wave_barrier();
    }

    int ol = lane;
    asm volatile("" : "+v"(ol));
#pragma unroll
    for (int r = 0; r < 4; ++r) {
      const int node = g * GROUP + 4 * (ol >> 4) + r;
      if (node < N) {
#pragma unroll
        for (int c = 0; c < 4; ++c) {
          const size_t off = (size_t)node * 512 + 128 * c + 8 * (ol & 15);
          const bf16x8 x = *reinterpret_cast<const bf16x8*>(sv + off);
          u32x4 o;
#pragma unroll
          for (int h = 0; h < 4; ++h) {
            const float a0 = bfx(x, 2 * h) + rb(acc[c][2 * h][r]);
            const float a1 = bfx(x, 2 * h + 1) + rb(acc[c][2 * h + 1][r]);
            o[h] = pack2(a0, a1);
          }
          *reinterpret_cast<u32x4*>(out + off) = o;
        }
      }
    }
    __builtin_amdgcn_wave_barrier();
  }
}

extern "C" void kernel_launch(void* const* d_in, const int* in_sizes, int n_in,
                              void* d_out, int out_size, void* d_ws, size_t ws_size,
                              hipStream_t stream) {
  const u16* sv = (const u16*)d_in[0];
  const u16* pos = (const u16*)d_in[1];
  const int* src = (const int*)d_in[2];
  const int* dst = (const int*)d_in[3];
  const u16* freqs = (const u16*)d_in[4];
  const u16* w1 = (const u16*)d_in[5];
  const u16* w2 = (const u16*)d_in[6];
  const u16* wr = (const u16*)d_in[7];
  const int N = in_sizes[0] / 512;
  const int E = in_sizes[2];
  const int ngroups = (N + GROUP - 1) / GROUP;
  const WsLayout L = ws_layout(N, ngroups, E);
  char* ws = (char*)d_ws;
  int* cnt = (int*)(ws + L.cnt);
  u16* w1f = (u16*)(ws + L.w1f);
  u16* wl = (u16*)(ws + L.wl);
  u16* hbuf = (u16*)(ws + L.h);
  int4* rec16 = (int4*)(ws + L.rec16);
  int* recd = (int*)(ws + L.recd);
  int4* ovf16 = (int4*)(ws + L.ovf16);
  int* ovfd = (int*)(ws + L.ovfd);
  int* ovfg = (int*)(ws + L.ovfg);

  const int prep_n = (71680 > ngroups + 1) ? 71680 : ngroups + 1;
  k_prep<<<(prep_n + 255) / 256, 256, 0, stream>>>(w1, w2, wr, cnt, w1f, wl, ngroups);
  const int h_blocks = (N + 63) / 64;
  const int edge_blocks = (E + 255) / 256;
  k_edges<<<h_blocks + edge_blocks, 256, 0, stream>>>(sv, pos, src, dst, w1f, cnt, hbuf, rec16, recd,
                                                       ovf16, ovfd, ovfg, N, E, ngroups, h_blocks);
  const size_t lds = WL_BYTES + K2_WAVES * SCR_BYTES;
  static_assert(WL_BYTES + K2_WAVES * SCR_BYTES <= 163840, "lds");
  int grid = 256;
  if (ngroups < grid) grid = ngroups;
  k_message<<<grid, 512, lds, stream>>>(sv, freqs, cnt, wl, hbuf, rec16, recd, ovf16, ovfd, ovfg,
                                        (u16*)d_out, N, ngroups);
}
```

```cpp
#include <hip/hip_runtime.h>
#include <hip/hip_bf16.h>
#include <stdint.h>

typedef unsigned short u16;
typedef unsigned int u32;
using bf16x8 = __attribute__((ext_vector_type(8))) short;
using bf16x4 = __attribute__((ext_vector_type(4))) short;
using f32x4 = __attribute__((ext_vector_type(4))) float;
using f32x2 = __attribute__((ext_vector_type(2))) float;
using u32x4 = __attribute__((ext_vector_type(4))) unsigned int;
using u32x2 = __attribute__((ext_vector_type(2))) unsigned int;

#define GROUP 16
#define CAP 128
#define CNT_STRIDE 32
#define W1_ELEMS 16384
#define W2_BYTES 98304
#define WR_BYTES 12288
#define WL_BYTES (W2_BYTES + WR_BYTES)
#define K2_WAVES 8
#define SCR_BYTES 2560

__device__ __forceinline__ float bf2f(u16 x) { return __uint_as_float(((u32)x) << 16); }
__device__ __forceinline__ u16 f2bf(float x) {
  __hip_bfloat16 b = __float2bfloat16(x);
  return *reinterpret_cast<u16*>(&b);
}
__device__ __forceinline__ float rb(float x) { return bf2f(f2bf(x)); }
typedef __bf16 hbf2 __attribute__((ext_vector_type(2)));
__device__ __forceinline__ u32 pack2(float lo, float hi) {
  f32x2 f = {lo, hi};
  hbf2 b = __builtin_convertvector(f, hbf2);
  return *reinterpret_cast<u32*>(&b);
}
__device__ __forceinline__ float lo16(u32 x) { return __uint_as_float(x << 16); }
__device__ __forceinline__ float hi16(u32 x) { return __uint_as_float(x & 0xffff0000u); }

struct WsLayout {
  size_t cnt, w1f, wl, pos4, h, rec16, recd, ovf16, ovfd, ovfg, total;
};
static inline size_t al256(size_t x) { return (x + 255) & ~(size_t)255; }
static inline WsLayout ws_layout(int N, int ngroups, int E) {
  WsLayout L;
  size_t o = 0;
  L.cnt = o;   o = al256(o + (size_t)(ngroups + 1) * CNT_STRIDE * 4);
  L.w1f = o;   o = al256(o + W1_ELEMS * 2);
  L.wl = o;    o = al256(o + WL_BYTES);
  L.pos4 = o;  o = al256(o + (size_t)N * 8);
  L.h = o;     o = al256(o + (size_t)N * 256);
  L.rec16 = o; o = al256(o + (size_t)ngroups * CAP * 16);
  L.recd = o;  o = al256(o + (size_t)ngroups * CAP * 4);
  L.ovf16 = o; o = al256(o + (size_t)E * 16);
  L.ovfd = o;  o = al256(o + (size_t)E * 4);
  L.ovfg = o;  o = al256(o + (size_t)E * 4);
  L.total = o;
  return L;
}

__global__ __launch_bounds__(256) void k_prep(const u16* __restrict__ w1, const u16* __restrict__ w2,
                                              const u16* __restrict__ wr, int* __restrict__ cnt,
                                              u16* __restrict__ w1f, u16* __restrict__ wl,
                                              const u16* __restrict__ pos, u32x2* __restrict__ pos4,
                                              int N, int ngroups) {
  const int idx = (int)blockIdx.x * 256 + threadIdx.x;
  if (idx < 16384) {
    int j = idx & 7, lane = (idx >> 3) & 63, ks = (idx >> 9) & 3, t = idx >> 11;
    int q = lane >> 4, n = lane & 15;
    w1f[idx] = w1[(32 * ks + 8 * q + j) * 128 + 16 * t + n];
  } else if (idx < 16384 + 49152) {
    int i = idx - 16384;
    int j = i & 7, lane = (i >> 3) & 63, ks = (i >> 9) & 3, st = i >> 11;
    int hf = st / 12, tp = (st % 12) / 3, c = st % 3;
    int q = lane >> 4, n = lane & 15;
    int u = 16 * (2 * ks + (j >> 2)) + 4 * q + (j & 3);
    wl[i] = w2[u * 384 + 128 * c + 64 * hf + 4 * n + tp];
  } else if (idx < 16384 + 49152 + 6144) {
    int i = idx - 16384 - 49152;
    int j = i & 3, lane = (i >> 2) & 63, st = i >> 8;
    int hf = st / 12, tp = (st % 12) / 3, c = st % 3;
    int q = lane >> 4, n = lane & 15;
    wl[49152 + i] = wr[(4 * q + j) * 384 + 128 * c + 64 * hf + 4 * n + tp];
  }
  if (idx <= ngroups) cnt[idx * CNT_STRIDE] = 0;
  if (idx < N) {
    u32x2 p;
    p[0] = (u32)pos[idx * 3] | ((u32)pos[idx * 3 + 1] << 16);
    p[1] = (u32)pos[idx * 3 + 2];
    pos4[idx] = p;
  }
}

__global__ __launch_bounds__(256) void k_edges(
    const u16* __restrict__ sv, const u32x2* __restrict__ pos4, const int* __restrict__ src,
    const int* __restrict__ dst, const u16* __restrict__ w1f, int* __restrict__ cnt,
    u16* __restrict__ hbuf, int4* __restrict__ rec16, int* __restrict__ recd,
    int4* __restrict__ ovf16, int* __restrict__ ovfd, int* __restrict__ ovfg, int N, int E,
    int ngroups, int h_blocks) {
  if ((int)blockIdx.x < h_blocks) {
    __shared__ __attribute__((aligned(16))) u32x4 w1s[2048];
    const int lane = threadIdx.x & 63, wave = threadIdx.x >> 6;
    const int q = lane >> 4, n = lane & 15;
    const int node0 = ((int)blockIdx.x * 4 + wave) * 32;
    {
      const u32x4* g4 = reinterpret_cast<const u32x4*>(w1f);
      u32x4 tmp[8];
#pragma unroll
      for (int k = 0; k < 8; ++k) tmp[k] = g4[threadIdx.x + k * 256];
      bf16x8 sB[2][4];
#pragma unroll
      for (int u = 0; u < 2; ++u) {
        int node = node0 + 16 * u + n;
        if (node >= N) node = N - 1;
        const bf16x8* srow = reinterpret_cast<const bf16x8*>(sv + (size_t)node * 512);
#pragma unroll
        for (int ks = 0; ks < 4; ++ks) sB[u][ks] = srow[4 * ks + q];
      }
      __builtin_amdgcn_sched_barrier(0);
#pragma unroll
      for (int k = 0; k < 8; ++k) w1s[threadIdx.x + k * 256] = tmp[k];
      __syncthreads();
      const bf16x8* W1f = reinterpret_cast<const bf16x8*>(w1s);
      u32x4 hp[2][4];
#pragma unroll
      for (int t = 0; t < 8; ++t) {
        f32x4 d1[2] = {{0.f, 0.f, 0.f, 0.f}, {0.f, 0.f, 0.f, 0.f}};
#pragma unroll
        for (int ks = 0; ks < 4; ++ks) {
          const bf16x8 wfr = W1f[(t * 4 + ks) * 64 + lane];
          d1[0] = __builtin_amdgcn_mfma_f32_16x16x32_bf16(wfr, sB[0][ks], d1[0], 0, 0, 0);
          d1[1] = __builtin_amdgcn_mfma_f32_16x16x32_bf16(wfr, sB[1][ks], d1[1], 0, 0, 0);
        }
#pragma unroll
        for (int u = 0; u < 2; ++u) {
          float sl[4];
#pragma unroll
          for (int r = 0; r < 4; ++r) {
            const float a = rb(d1[u][r]);
            sl[r] = a * __builtin_amdgcn_rcpf(1.0f + __expf(-a));
          }
          hp[u][t >> 1][(t & 1) * 2 + 0] = pack2(sl[0], sl[1]);
          hp[u][t >> 1][(t & 1) * 2 + 1] = pack2(sl[2], sl[3]);
        }
      }
#pragma unroll
      for (int u = 0; u < 2; ++u) {
        const int node = node0 + 16 * u + n;
        if (node < N) {
          u32x4* hrow = reinterpret_cast<u32x4*>(hbuf + (size_t)node * 128);
#pragma unroll
          for (int ks = 0; ks < 4; ++ks) hrow[4 * ks + q] = hp[u][ks];
        }
      }
    }
    return;
  }
  const int e = ((int)blockIdx.x - h_blocks) * 256 + threadIdx.x;
  if (e >= E) return;
  const int s = src[e], d = dst[e];
  const u32x2 ps = pos4[s], pd = pos4[d];
  const float rx = rb(lo16(ps[0]) - lo16(pd[0]));
  const float ry = rb(hi16(ps[0]) - hi16(pd[0]));
  const float rz = rb(lo16(ps[1]) - lo16(pd[1]));
  float q2 = rb(rb(rx * rx) + rb(ry * ry) + rb(rz * rz));
  q2 = rb(q2 + 1e-8f);
  const float dd = rb(sqrtf(q2));
  if (!(dd < 5.0f)) return;
  const float hx = rb(rx / dd), hy = rb(ry / dd), hz = rb(rz / dd);
  const float fc = rb(0.5f * rb(rb(cosf(rb(rb(3.141592653589793f * dd) / 5.0f))) + 1.0f));
  const int g = d >> 4;
  int4 r;
  r.x = (e << 4) | (d & 15);
  r.y = s;
  r.z = (int)((u32)f2bf(hx) | ((u32)f2bf(hy) << 16));
  r.w = (int)((u32)f2bf(hz) | ((u32)f2bf(fc) << 16));
  const int dbits = (int)__float_as_uint(dd);
  const int slot = atomicAdd(&cnt[g * CNT_STRIDE], 1);
  if (slot < CAP) {
    rec16[(size_t)g * CAP + slot] = r;
    recd[(size_t)g * CAP + slot] = dbits;
  } else {
    const int o = atomicAdd(&cnt[ngroups * CNT_STRIDE], 1);
    ovf16[o] = r;
    ovfd[o] = dbits;
    ovfg[o] = g;
  }
}

__device__ __forceinline__ float bfx(const bf16x8& v, int i) {
  return __uint_as_float(((u32)(u16)v[i]) << 16);
}
__device__ __forceinline__ bf16x4 pack4(float a, float b, float c, float d) {
  u32x2 p;
  p[0] = pack2(a, b);
  p[1] = pack2(c, d);
  return *reinterpret_cast<bf16x4*>(&p);
}

__global__ __launch_bounds__(512) void k_message(
    const u16* __restrict__ sv, const u16* __restrict__ freqs, const int* __restrict__ cnt,
    const u16* __restrict__ wl_g, const u16* __restrict__ hbuf, const int4* __restrict__ rec16,
    const int* __restrict__ recd, const int4* __restrict__ ovf16, const int* __restrict__ ovfd,
    const int* __restrict__ ovfg, u16* __restrict__ out, int N, int ngroups) {
  extern __shared__ __attribute__((aligned(16))) char smem[];
  const int tid = threadIdx.x;
  const int lane = tid & 63, wave = tid >> 6;

  {
    const u32x4* g4 = reinterpret_cast<const u32x4*>(wl_g);
    u32x4* l4 = reinterpret_cast<u32x4*>(smem);
    constexpr int NST = WL_BYTES / 16;
    constexpr int NIT = (NST + 511) / 512;
    u32x4 tmp[NIT];
#pragma unroll
    for (int k = 0; k < NIT; ++k) {
      const int i = tid + k * 512;
      if (i < NST) tmp[k] = g4[i];
    }
    __builtin_amdgcn_sched_barrier(0);
#pragma unroll
    for (int k = 0; k < NIT; ++k) {
      const int i = tid + k * 512;
      if (i < NST) l4[i] = tmp[k];
    }
  }
  const bf16x8* W2f = reinterpret_cast<const bf16x8*>(smem);
  const bf16x4* Wrf = reinterpret_cast<const bf16x4*>(smem + W2_BYTES);
  int* scr = reinterpret_cast<int*>(smem + WL_BYTES + wave * SCR_BYTES);
  int* s_key = scr;
  int* s_src = scr + 128;
  int* s_hxy = scr + 256;
  int* s_hzf = scr + 384;
  int* s_d = scr + 512;

  __syncthreads();

  for (int g = (int)blockIdx.x + (int)gridDim.x * wave; g < ngroups; g += (int)gridDim.x * K2_WAVES) {
    const int total = cnt[g * CNT_STRIDE];
    const int nb = total < CAP ? total : CAP;
    const int ovf_n = (total > CAP) ? cnt[ngroups * CNT_STRIDE] : 0;

#pragma unroll 1
    for (int hf = 0; hf < 2; ++hf) {
      {
        int bl = lane;
        asm volatile("" : "+v"(bl));
        int4 r0 = rec16[(size_t)g * CAP + bl], r1 = rec16[(size_t)g * CAP + bl + 64];
        int d0 = recd[(size_t)g * CAP + bl], d1 = recd[(size_t)g * CAP + bl + 64];
        if (bl >= nb) r0.x = 0x7fffffff;
        if (bl + 64 >= nb) r1.x = 0x7fffffff;
        s_key[bl] = r0.x;
        s_key[bl + 64] = r1.x;
        __builtin_amdgcn_wave_barrier();
        int k0 = 0, k1 = 0;
        for (int i = 0; i < nb; i += 4) {
          int4 kk = *reinterpret_cast<const int4*>(s_key + i);
          k0 += (kk.x < r0.x) + (kk.y < r0.x) + (kk.z < r0.x) + (kk.w < r0.x);
          k1 += (kk.x < r1.x) + (kk.y < r1.x) + (kk.z < r1.x) + (kk.w < r1.x);
        }
        __builtin_amdgcn_wave_barrier();
        if (bl < nb) { s_key[k0] = r0.x; s_src[k0] = r0.y; s_hxy[k0] = r0.z; s_hzf[k0] = r0.w; s_d[k0] = d0; }
        if (bl + 64 < nb) { s_key[k1] = r1.x; s_src[k1] = r1.y; s_hxy[k1] = r1.z; s_hzf[k1] = r1.w; s_d[k1] = d1; }
        __builtin_amdgcn_wave_barrier();
      }

      f32x4 acc[4][4];
#pragma unroll
      for (int c = 0; c < 4; ++c)
#pragma unroll
        for (int t = 0; t < 4; ++t) acc[c][t] = (f32x4){0.f, 0.f, 0.f, 0.f};

      int have = nb;
      int ovf_pos = 0;

      while (true) {
        for (int base = 0; base < have; base += 16) {
          int wl = lane;
          asm volatile("" : "+v"(wl));
          const int q = wl >> 4, n = wl & 15;
          const int ie = base + n;
          const bool ve = ie < have;
          const int src_e = ve ? s_src[ie] : 0;
          const float d_e = ve ? __int_as_float(s_d[ie]) : 1.0f;
          int src4[4], dl4[4];
          u32 hxy4[4], hzf4[4];
#pragma unroll
          for (int r = 0; r < 4; ++r) {
            const int i4 = base + 4 * q + r;
            const bool v4 = i4 < have;
            src4[r] = v4 ? s_src[i4] : 0;
            dl4[r] = v4 ? (s_key[i4] & 15) : -1;
            hxy4[r] = v4 ? (u32)s_hxy[i4] : 0u;
            hzf4[r] = v4 ? (u32)s_hzf[i4] : 0u;
          }
          bf16x8 hA[4];
          {
            const bf16x8* hrow = reinterpret_cast<const bf16x8*>(hbuf + (size_t)src_e * 128);
#pragma unroll
            for (int ks = 0; ks < 4; ++ks) hA[ks] = hrow[4 * ks + q];
          }
          u32x2 vr[4][3];
#pragma unroll
          for (int r = 0; r < 4; ++r) {
            const u32x2* vrow = reinterpret_cast<const u32x2*>(sv + (size_t)src4[r] * 512 + 128 + 64 * hf);
#pragma unroll
            for (int x = 0; x < 3; ++x) vr[r][x] = vrow[32 * x + n];
          }
          const bf16x8* W2h = W2f + (size_t)hf * 12 * 4 * 64 + wl;
          const bf16x4* Wrh = Wrf + (size_t)hf * 12 * 64 + wl;
          bf16x8 wb[2][4];
          bf16x4 wrb[2];
#pragma unroll
          for (int ks = 0; ks < 4; ++ks) wb[0][ks] = W2h[ks * 64];
          wrb[0] = Wrh[0];
          bf16x4 rbfA;
          {
            float rv[4];
            const u32x2 fq = *reinterpret_cast<const u32x2*>(freqs + 4 * q);
            const float fk[4] = {lo16(fq[0]), hi16(fq[0]), lo16(fq[1]), hi16(fq[1])};
#pragma unroll
            for (int j = 0; j < 4; ++j) {
              const float a = rb(d_e * fk[j]);
              const float sn = rb(__sinf(a));
              rv[j] = sn / d_e;
            }
            rbfA = pack4(rv[0], rv[1], rv[2], rv[3]);
          }
          bf16x4 selA;
#pragma unroll
          for (int j = 0; j < 4; ++j) selA[j] = (dl4[j] == n) ? (short)(hzf4[j] >> 16) : (short)0;
          float hat[3][4];
#pragma unroll
          for (int r = 0; r < 4; ++r) {
            hat[0][r] = lo16(hxy4[r]); hat[1][r] = hi16(hxy4[r]); hat[2][r] = lo16(hzf4[r]);
          }
          __builtin_amdgcn_sched_barrier(0);

          f32x4 m[3];
#pragma unroll
          for (int st = 0; st < 12; ++st) {
            const int tp = st / 3, c = st % 3, cur = st & 1;
            if (st + 1 < 12) {
#pragma unroll
              for (int ks = 0; ks < 4; ++ks) wb[cur ^ 1][ks] = W2h[((st + 1) * 4 + ks) * 64];
              wrb[cur ^ 1] = Wrh[(st + 1) * 64];
            }
            __builtin_amdgcn_sched_barrier(0);
            f32x4 d2 = {0.f, 0.f, 0.f, 0.f};
#pragma unroll
            for (int ks = 0; ks < 4; ++ks)
              d2 = __builtin_amdgcn_mfma_f32_16x16x32_bf16(hA[ks], wb[cur][ks], d2, 0, 0, 0);
            f32x4 wf = {0.f, 0.f, 0.f, 0.f};
            wf = __builtin_amdgcn_mfma_f32_16x16x16bf16_1k(rbfA, wrb[cur], wf, 0, 0, 0);
#pragma unroll
            for (int r = 0; r < 4; ++r) m[c][r] = d2[r] * wf[r];
            if (c == 2) {
              acc[0][tp] = __builtin_amdgcn_mfma_f32_16x16x16bf16_1k(
                  selA, pack4(m[0][0], m[0][1], m[0][2], m[0][3]), acc[0][tp], 0, 0, 0);
#pragma unroll
              for (int x = 0; x < 3; ++x) {
                float mv[4];
#pragma unroll
                for (int r = 0; r < 4; ++r) {
                  const u32 w = vr[r][x][tp >> 1];
                  const float vv = (tp & 1) ? hi16(w) : lo16(w);
                  mv[r] = m[1][r] * vv + m[2][r] * hat[x][r];
                }
                acc[1 + x][tp] = __builtin_amdgcn_mfma_f32_16x16x16bf16_1k(
                    selA, pack4(mv[0], mv[1], mv[2], mv[3]), acc[1 + x][tp], 0, 0, 0);
              }
            }
            __builtin_amdgcn_sched_barrier(0);
          }
        }
        if (ovf_pos >= ovf_n) break;
        __builtin_amdgcn_wave_barrier();
        have = 0;
        while (ovf_pos < ovf_n && have <= CAP - 64) {
          const int i = ovf_pos + lane;
          const bool mine = (i < ovf_n) && (ovfg[i] == g);
          const unsigned long long bal = __ballot(mine);
          const int before = __popcll(bal & ((1ull << lane) - 1ull));
          if (mine) {
            const int4 r = ovf16[i];
            const int p = have + before;
            s_key[p] = r.x; s_src[p] = r.y; s_hxy[p] = r.z; s_hzf[p] = r.w; s_d[p] = ovfd[i];
          }
          have += __popcll(bal);
          ovf_pos += 64;
        }
        __builtin_amdgcn_wave_barrier();
      }

      int ol = lane;
      asm volatile("" : "+v"(ol));
#pragma unroll
      for (int r = 0; r < 4; ++r) {
        const int node = g * GROUP + 4 * (ol >> 4) + r;
        if (node < N) {
#pragma unroll
          for (int c = 0; c < 4; ++c) {
            const size_t off = (size_t)node * 512 + 128 * c + 64 * hf + 4 * (ol & 15);
            const u32x2 x = *reinterpret_cast<const u32x2*>(sv + off);
            u32x2 o;
            o[0] = pack2(lo16(x[0]) + rb(acc[c][0][r]), hi16(x[0]) + rb(acc[c][1][r]));
            o[1] = pack2(lo16(x[1]) + rb(acc[c][2][r]), hi16(x[1]) + rb(acc[c][3][r]));
            *reinterpret_cast<u32x2*>(out + off) = o;
          }
        }
      }
      __builtin_amdgcn_wave_barrier();
    }
  }
}

extern "C" void kernel_launch(void* const* d_in, const int* in_sizes, int n_in,
                              void* d_out, int out_size, void* d_ws, size_t ws_size,
                              hipStream_t stream) {
  const u16* sv = (const u16*)d_in[0];
  const u16* pos = (const u16*)d_in[1];
  const int* src = (const int*)d_in[2];
  const int* dst = (const int*)d_in[3];
  const u16* freqs = (const u16*)d_in[4];
  const u16* w1 = (const u16*)d_in[5];
  const u16* w2 = (const u16*)d_in[6];
  const u16* wr = (const u16*)d_in[7];
  const int N = in_sizes[0] / 512;
  const int E = in_sizes[2];
  const int ngroups = (N + GROUP - 1) / GROUP;
  const WsLayout L = ws_layout(N, ngroups, E);
  char* ws = (char*)d_ws;
  int* cnt = (int*)(ws + L.cnt);
  u16* w1f = (u16*)(ws + L.w1f);
  u16* wl = (u16*)(ws + L.wl);
  u32x2* pos4 = (u32x2*)(ws + L.pos4);
  u16* hbuf = (u16*)(ws + L.h);
  int4* rec16 = (int4*)(ws + L.rec16);
  int* recd = (int*)(ws + L.recd);
  int4* ovf16 = (int4*)(ws + L.ovf16);
  int* ovfd = (int*)(ws + L.ovfd);
  int* ovfg = (int*)(ws + L.ovfg);

  int prep_n = (71680 > ngroups + 1) ? 71680 : ngroups + 1;
  if (N > prep_n) prep_n = N;
  k_prep<<<(prep_n + 255) / 256, 256, 0, stream>>>(w1, w2, wr, cnt, w1f, wl, pos, pos4, N, ngroups);
  const int h_blocks = (N + 127) / 128;
  const int edge_blocks = (E + 255) / 256;
  k_edges<<<h_blocks + edge_blocks, 256, 0, stream>>>(sv, pos4, src, dst, w1f, cnt, hbuf, rec16, recd,
                                                       ovf16, ovfd, ovfg, N, E, ngroups, h_blocks);
  const size_t lds = WL_BYTES + K2_WAVES * SCR_BYTES;
  static_assert(WL_BYTES + K2_WAVES * SCR_BYTES <= 163840, "lds");
  int grid = 256;
  if (ngroups < grid) grid = ngroups;
  k_message<<<grid, 512, lds, stream>>>(sv, freqs, cnt, wl, hbuf, rec16, recd, ovf16, ovfd, ovfg,
                                        (u16*)d_out, N, ngroups);
}
```

```cpp
#include <hip/hip_runtime.h>
#include <hip/hip_bf16.h>
#include <stdint.h>

typedef unsigned short u16;
typedef unsigned int u32;
using bf16x8 = __attribute__((ext_vector_type(8))) short;
using bf16x4 = __attribute__((ext_vector_type(4))) short;
using f32x4 = __attribute__((ext_vector_type(4))) float;
using f32x2 = __attribute__((ext_vector_type(2))) float;
using u32x4 = __attribute__((ext_vector_type(4))) unsigned int;
using u32x2 = __attribute__((ext_vector_type(2))) unsigned int;

#define GROUP 16
#define CAP 128
#define CNT_STRIDE 32
#define W1_ELEMS 16384
#define W2_BYTES 98304
#define WR_BYTES 12288
#define WL_BYTES (W2_BYTES + WR_BYTES)
#define K2_WAVES 8
#define SCR_BYTES 2560

__device__ __forceinline__ float bf2f(u16 x) { return __uint_as_float(((u32)x) << 16); }
__device__ __forceinline__ u16 f2bf(float x) {
  __hip_bfloat16 b = __float2bfloat16(x);
  return *reinterpret_cast<u16*>(&b);
}
__device__ __forceinline__ float rb(float x) { return bf2f(f2bf(x)); }
typedef __bf16 hbf2 __attribute__((ext_vector_type(2)));
__device__ __forceinline__ u32 pack2(float lo, float hi) {
  f32x2 f = {lo, hi};
  hbf2 b = __builtin_convertvector(f, hbf2);
  return *reinterpret_cast<u32*>(&b);
}
__device__ __forceinline__ float lo16(u32 x) { return __uint_as_float(x << 16); }
__device__ __forceinline__ float hi16(u32 x) { return __uint_as_float(x & 0xffff0000u); }

struct WsLayout {
  size_t cnt, w1f, wl, pos4, h, rec16, rbf, ovf16, ovfg, total;
};
static inline size_t al256(size_t x) { return (x + 255) & ~(size_t)255; }
static inline WsLayout ws_layout(int N, int ngroups, int E) {
  WsLayout L;
  size_t o = 0;
  L.cnt = o;   o = al256(o + (size_t)(ngroups + 1) * CNT_STRIDE * 4);
  L.w1f = o;   o = al256(o + W1_ELEMS * 2);
  L.wl = o;    o = al256(o + WL_BYTES);
  L.pos4 = o;  o = al256(o + (size_t)N * 8);
  L.h = o;     o = al256(o + (size_t)N * 256);
  L.rec16 = o; o = al256(o + (size_t)ngroups * CAP * 16);
  L.rbf = o;   o = al256(o + ((size_t)ngroups * CAP + (size_t)E) * 32);
  L.ovf16 = o; o = al256(o + (size_t)E * 16);
  L.ovfg = o;  o = al256(o + (size_t)E * 4);
  L.total = o;
  return L;
}

__global__ __launch_bounds__(256) void k_prep(const u16* __restrict__ w1, const u16* __restrict__ w2,
                                              const u16* __restrict__ wr, int* __restrict__ cnt,
                                              u16* __restrict__ w1f, u16* __restrict__ wl,
                                              const u16* __restrict__ pos, u32x2* __restrict__ pos4,
                                              int N, int ngroups) {
  const int idx = (int)blockIdx.x * 256 + threadIdx.x;
  if (idx < 16384) {
    int j = idx & 7, lane = (idx >> 3) & 63, ks = (idx >> 9) & 3, t = idx >> 11;
    int q = lane >> 4, n = lane & 15;
    w1f[idx] = w1[(32 * ks + 8 * q + j) * 128 + 16 * t + n];
  } else if (idx < 16384 + 49152) {
    int i = idx - 16384;
    int j = i & 7, lane = (i >> 3) & 63, ks = (i >> 9) & 3, st = i >> 11;
    int hf = st / 12, tp = (st % 12) / 3, c = st % 3;
    int q = lane >> 4, n = lane & 15;
    int u = 16 * (2 * ks + (j >> 2)) + 4 * q + (j & 3);
    wl[i] = w2[u * 384 + 128 * c + 64 * hf + 4 * n + tp];
  } else if (idx < 16384 + 49152 + 6144) {
    int i = idx - 16384 - 49152;
    int j = i & 3, lane = (i >> 2) & 63, st = i >> 8;
    int hf = st / 12, tp = (st % 12) / 3, c = st % 3;
    int q = lane >> 4, n = lane & 15;
    wl[49152 + i] = wr[(4 * q + j) * 384 + 128 * c + 64 * hf + 4 * n + tp];
  }
  if (idx <= ngroups) cnt[idx * CNT_STRIDE] = 0;
  if (idx < N) {
    u32x2 p;
    p[0] = (u32)pos[idx * 3] | ((u32)pos[idx * 3 + 1] << 16);
    p[1] = (u32)pos[idx * 3 + 2];
    pos4[idx] = p;
  }
}

__global__ __launch_bounds__(256) void k_edges(
    const u16* __restrict__ sv, const u32x2* __restrict__ pos4, const int* __restrict__ src,
    const int* __restrict__ dst, const u16* __restrict__ w1f, int* __restrict__ cnt,
    u16* __restrict__ hbuf, const u16* __restrict__ freqs, int4* __restrict__ rec16,
    u32x4* __restrict__ rbfo, int4* __restrict__ ovf16, int* __restrict__ ovfg, int N, int E,
    int ngroups, int h_blocks) {
  if ((int)blockIdx.x < h_blocks) {
    __shared__ __attribute__((aligned(16))) u32x4 w1s[2048];
    const int lane = threadIdx.x & 63, wave = threadIdx.x >> 6;
    const int q = lane >> 4, n = lane & 15;
    const int node0 = ((int)blockIdx.x * 4 + wave) * 32;
    {
      const u32x4* g4 = reinterpret_cast<const u32x4*>(w1f);
      u32x4 tmp[8];
#pragma unroll
      for (int k = 0; k < 8; ++k) tmp[k] = g4[threadIdx.x + k * 256];
      bf16x8 sB[2][4];
#pragma unroll
      for (int u = 0; u < 2; ++u) {
        int node = node0 + 16 * u + n;
        if (node >= N) node = N - 1;
        const bf16x8* srow = reinterpret_cast<const bf16x8*>(sv + (size_t)node * 512);
#pragma unroll
        for (int ks = 0; ks < 4; ++ks) sB[u][ks] = srow[4 * ks + q];
      }
      __builtin_amdgcn_sched_barrier(0);
#pragma unroll
      for (int k = 0; k < 8; ++k) w1s[threadIdx.x + k * 256] = tmp[k];
      __syncthreads();
      const bf16x8* W1f = reinterpret_cast<const bf16x8*>(w1s);
      u32x4 hp[2][4];
#pragma unroll
      for (int t = 0; t < 8; ++t) {
        f32x4 d1[2] = {{0.f, 0.f, 0.f, 0.f}, {0.f, 0.f, 0.f, 0.f}};
#pragma unroll
        for (int ks = 0; ks < 4; ++ks) {
          const bf16x8 wfr = W1f[(t * 4 + ks) * 64 + lane];
          d1[0] = __builtin_amdgcn_mfma_f32_16x16x32_bf16(wfr, sB[0][ks], d1[0], 0, 0, 0);
          d1[1] = __builtin_amdgcn_mfma_f32_16x16x32_bf16(wfr, sB[1][ks], d1[1], 0, 0, 0);
        }
#pragma unroll
        for (int u = 0; u < 2; ++u) {
          float sl[4];
#pragma unroll
          for (int r = 0; r < 4; ++r) {
            const float a = rb(d1[u][r]);
            sl[r] = a * __builtin_amdgcn_rcpf(1.0f + __expf(-a));
          }
          hp[u][t >> 1][(t & 1) * 2 + 0] = pack2(sl[0], sl[1]);
          hp[u][t >> 1][(t & 1) * 2 + 1] = pack2(sl[2], sl[3]);
        }
      }
#pragma unroll
      for (int u = 0; u < 2; ++u) {
        const int node = node0 + 16 * u + n;
        if (node < N) {
          u32x4* hrow = reinterpret_cast<u32x4*>(hbuf + (size_t)node * 128);
#pragma unroll
          for (int ks = 0; ks < 4; ++ks) hrow[4 * ks + q] = hp[u][ks];
        }
      }
    }
    return;
  }
  const int e = ((int)blockIdx.x - h_blocks) * 256 + threadIdx.x;
  if (e >= E) return;
  const int s = src[e], d = dst[e];
  const u32x2 ps = pos4[s], pd = pos4[d];
  const float rx = rb(lo16(ps[0]) - lo16(pd[0]));
  const float ry = rb(hi16(ps[0]) - hi16(pd[0]));
  const float rz = rb(lo16(ps[1]) - lo16(pd[1]));
  float q2 = rb(rb(rx * rx) + rb(ry * ry) + rb(rz * rz));
  q2 = rb(q2 + 1e-8f);
  const float dd = rb(sqrtf(q2));
  if (!(dd < 5.0f)) return;
  const float hx = rb(rx / dd), hy = rb(ry / dd), hz = rb(rz / dd);
  const float fc = rb(0.5f * rb(rb(cosf(rb(rb(3.141592653589793f * dd) / 5.0f))) + 1.0f));
  const int g = d >> 4;
  int4 r;
  r.x = (e << 4) | (d & 15);
  r.y = s;
  r.z = (int)((u32)f2bf(hx) | ((u32)f2bf(hy) << 16));
  r.w = (int)((u32)f2bf(hz) | ((u32)f2bf(fc) << 16));
  const int slot = atomicAdd(&cnt[g * CNT_STRIDE], 1);
  u32x4 rq[2];
#pragma unroll
  for (int k = 0; k < 8; ++k) {
    const u32 fq = *reinterpret_cast<const u32*>(freqs + 2 * k);
    const float s0 = rb(__sinf(rb(dd * lo16(fq))));
    const float s1 = rb(__sinf(rb(dd * hi16(fq))));
    rq[k >> 2][k & 3] = pack2(s0 / dd, s1 / dd);
  }
  size_t ridx;
  if (slot < CAP) {
    ridx = (size_t)g * CAP + slot;
    rec16[ridx] = r;
  } else {
    const int o = atomicAdd(&cnt[ngroups * CNT_STRIDE], 1);
    ovf16[o] = r;
    ovfg[o] = g;
    ridx = (size_t)ngroups * CAP + o;
  }
  rbfo[ridx * 2] = rq[0];
  rbfo[ridx * 2 + 1] = rq[1];
}

__device__ __forceinline__ float bfx(const bf16x8& v, int i) {
  return __uint_as_float(((u32)(u16)v[i]) << 16);
}
__device__ __forceinline__ bf16x4 pack4(float a, float b, float c, float d) {
  u32x2 p;
  p[0] = pack2(a, b);
  p[1] = pack2(c, d);
  return *reinterpret_cast<bf16x4*>(&p);
}

__global__ __launch_bounds__(512) void k_message(
    const u16* __restrict__ sv, const int* __restrict__ cnt, const u16* __restrict__ wl_g,
    const u16* __restrict__ hbuf, const int4* __restrict__ rec16, const u16* __restrict__ rbfa,
    const int4* __restrict__ ovf16, const int* __restrict__ ovfg, u16* __restrict__ out, int N,
    int ngroups) {
  extern __shared__ __attribute__((aligned(16))) char smem[];
  const int tid = threadIdx.x;
  const int lane = tid & 63, wave = tid >> 6;

  {
    const u32x4* g4 = reinterpret_cast<const u32x4*>(wl_g);
    u32x4* l4 = reinterpret_cast<u32x4*>(smem);
    constexpr int NST = WL_BYTES / 16;
    constexpr int NIT = (NST + 511) / 512;
    u32x4 tmp[NIT];
#pragma unroll
    for (int k = 0; k < NIT; ++k) {
      const int i = tid + k * 512;
      if (i < NST) tmp[k] = g4[i];
    }
    __builtin_amdgcn_sched_barrier(0);
#pragma unroll
    for (int k = 0; k < NIT; ++k) {
      const int i = tid + k * 512;
      if (i < NST) l4[i] = tmp[k];
    }
  }
  const bf16x8* W2f = reinterpret_cast<const bf16x8*>(smem);
  const bf16x4* Wrf = reinterpret_cast<const bf16x4*>(smem + W2_BYTES);
  int* scr = reinterpret_cast<int*>(smem + WL_BYTES + wave * SCR_BYTES);
  int* s_key = scr;
  int* s_src = scr + 128;
  int* s_hxy = scr + 256;
  int* s_hzf = scr + 384;
  int* s_ri = scr + 512;

  __syncthreads();

  const char* svb = reinterpret_cast<const char*>(sv);
  const char* hb = reinterpret_cast<const char*>(hbuf);
  const char* rbb = reinterpret_cast<const char*>(rbfa);

  for (int k = wave; (int)blockIdx.x + (int)gridDim.x * (k >> 1) < ngroups; k += K2_WAVES) {
    const int g = (int)blockIdx.x + (int)gridDim.x * (k >> 1);
    const int hf = k & 1;
    const int total = cnt[g * CNT_STRIDE];
    const int nb = total < CAP ? total : CAP;
    const int ovf_n = (total > CAP) ? cnt[ngroups * CNT_STRIDE] : 0;

    {
      int bl = lane;
      asm volatile("" : "+v"(bl));
      int4 r0 = rec16[(size_t)g * CAP + bl], r1 = rec16[(size_t)g * CAP + bl + 64];
      if (bl >= nb) r0.x = 0x7fffffff;
      if (bl + 64 >= nb) r1.x = 0x7fffffff;
      s_key[bl] = r0.x;
      s_key[bl + 64] = r1.x;
      __builtin_amdgcn_wave_barrier();
      int k0 = 0, k1 = 64;
      for (int i = 0; i < nb; i += 4) {
        int4 kk = *reinterpret_cast<const int4*>(s_key + i);
        k0 += (kk.x < r0.x) + (kk.y < r0.x) + (kk.z < r0.x) + (kk.w < r0.x);
      }
      if (nb > 64) {
        k1 = 0;
        for (int i = 0; i < nb; i += 4) {
          int4 kk = *reinterpret_cast<const int4*>(s_key + i);
          k1 += (kk.x < r1.x) + (kk.y < r1.x) + (kk.z < r1.x) + (kk.w < r1.x);
        }
      }
      if (bl >= nb) { k0 = bl; r0 = make_int4(-1, 0, 0, 0); }
      if (bl + 64 >= nb) { k1 = bl + 64; r1 = make_int4(-1, 0, 0, 0); }
      __builtin_amdgcn_wave_barrier();
      s_key[k0] = r0.x; s_src[k0] = r0.y; s_hxy[k0] = r0.z; s_hzf[k0] = r0.w; s_ri[k0] = g * CAP + (bl < nb ? bl : 0);
      s_key[k1] = r1.x; s_src[k1] = r1.y; s_hxy[k1] = r1.z; s_hzf[k1] = r1.w; s_ri[k1] = g * CAP + (bl + 64 < nb ? bl + 64 : 0);
      __builtin_amdgcn_wave_barrier();
    }

    f32x4 acc[4][4];
#pragma unroll
    for (int c = 0; c < 4; ++c)
#pragma unroll
      for (int t = 0; t < 4; ++t) acc[c][t] = (f32x4){0.f, 0.f, 0.f, 0.f};

    int have = nb;
    int ovf_pos = 0;

    struct Tile {
      bf16x8 hA[4];
      u32x2 vr[4][3];
      bf16x4 rbfA;
      int4 key4, hxy4, hzf4;
    };
    auto fetch = [&](int base, Tile& T) {
      int fl = lane;
      asm volatile("" : "+v"(fl));
      const int q_ = fl >> 4, n_ = fl & 15;
      const u32 src_e = (u32)s_src[base + n_];
      const u32 ri_e = (u32)s_ri[base + n_];
      const int4 src4 = *reinterpret_cast<const int4*>(s_src + base + 4 * q_);
      T.key4 = *reinterpret_cast<const int4*>(s_key + base + 4 * q_);
      T.hxy4 = *reinterpret_cast<const int4*>(s_hxy + base + 4 * q_);
      T.hzf4 = *reinterpret_cast<const int4*>(s_hzf + base + 4 * q_);
      const u32 ho = src_e * 256u + (u32)q_ * 16u;
#pragma unroll
      for (int ks = 0; ks < 4; ++ks) T.hA[ks] = *reinterpret_cast<const bf16x8*>(hb + (ho + 64u * ks));
      T.rbfA = *reinterpret_cast<const bf16x4*>(rbb + ((size_t)ri_e * 32u + (u32)q_ * 8u));
      const u32 vo = 256u + 128u * (u32)hf + 8u * (u32)n_;
      const u32 s4[4] = {(u32)src4.x, (u32)src4.y, (u32)src4.z, (u32)src4.w};
#pragma unroll
      for (int r = 0; r < 4; ++r) {
        const u32 o = s4[r] * 1024u + vo;
#pragma unroll
        for (int x = 0; x < 3; ++x) T.vr[r][x] = *reinterpret_cast<const u32x2*>(svb + (o + 256u * x));
      }
    };
    auto compute = [&](const Tile& T) {
      int wl = lane;
      asm volatile("" : "+v"(wl));
      const int n = wl & 15;
      const bf16x8* W2h = W2f + (size_t)hf * 12 * 4 * 64 + wl;
      const bf16x4* Wrh = Wrf + (size_t)hf * 12 * 64 + wl;
      bf16x8 wb[2][4];
      bf16x4 wrb[2];
#pragma unroll
      for (int ks = 0; ks < 4; ++ks) wb[0][ks] = W2h[ks * 64];
      wrb[0] = Wrh[0];
      const int key4[4] = {T.key4.x, T.key4.y, T.key4.z, T.key4.w};
      const u32 hxy4[4] = {(u32)T.hxy4.x, (u32)T.hxy4.y, (u32)T.hxy4.z, (u32)T.hxy4.w};
      const u32 hzf4[4] = {(u32)T.hzf4.x, (u32)T.hzf4.y, (u32)T.hzf4.z, (u32)T.hzf4.w};
      bf16x4 selA;
#pragma unroll
      for (int j = 0; j < 4; ++j)
        selA[j] = (key4[j] >= 0 && (key4[j] & 15) == n) ? (short)(hzf4[j] >> 16) : (short)0;
      float hat[3][4];
#pragma unroll
      for (int r = 0; r < 4; ++r) {
        hat[0][r] = lo16(hxy4[r]); hat[1][r] = hi16(hxy4[r]); hat[2][r] = lo16(hzf4[r]);
      }
      __builtin_amdgcn_sched_barrier(0);
      f32x4 m[3];
#pragma unroll
      for (int st = 0; st < 12; ++st) {
        const int tp = st / 3, c = st % 3, cur = st & 1;
        if (st + 1 < 12) {
#pragma unroll
          for (int ks = 0; ks < 4; ++ks) wb[cur ^ 1][ks] = W2h[((st + 1) * 4 + ks) * 64];
          wrb[cur ^ 1] = Wrh[(st + 1) * 64];
        }
        __builtin_amdgcn_sched_barrier(0);
        f32x4 d2 = {0.f, 0.f, 0.f, 0.f};
#pragma unroll
        for (int ks = 0; ks < 4; ++ks)
          d2 = __builtin_amdgcn_mfma_f32_16x16x32_bf16(T.hA[ks], wb[cur][ks], d2, 0, 0, 0);
        f32x4 wf = {0.f, 0.f, 0.f, 0.f};
        wf = __builtin_amdgcn_mfma_f32_16x16x16bf16_1k(T.rbfA, wrb[cur], wf, 0, 0, 0);
#pragma unroll
        for (int r = 0; r < 4; ++r) m[c][r] = d2[r] * wf[r];
        if (c == 2) {
          acc[0][tp] = __builtin_amdgcn_mfma_f32_16x16x16bf16_1k(
              selA, pack4(m[0][0], m[0][1], m[0][2], m[0][3]), acc[0][tp], 0, 0, 0);
#pragma unroll
          for (int x = 0; x < 3; ++x) {
            float mv[4];
#pragma unroll
            for (int r = 0; r < 4; ++r) {
              const u32 w = T.vr[r][x][tp >> 1];
              const float vv = (tp & 1) ? hi16(w) : lo16(w);
              mv[r] = m[1][r] * vv + m[2][r] * hat[x][r];
            }
            acc[1 + x][tp] = __builtin_amdgcn_mfma_f32_16x16x16bf16_1k(
                selA, pack4(mv[0], mv[1], mv[2], mv[3]), acc[1 + x][tp], 0, 0, 0);
          }
        }
        __builtin_amdgcn_sched_barrier(0);
      }
    };

    while (true) {
      Tile TA, TB;
      if (have > 0) fetch(0, TA);
      for (int base = 0; base < have; base += 32) {
        if (base + 16 < have) fetch(base + 16, TB);
        __builtin_amdgcn_sched_barrier(0);
        compute(TA);
        if (base + 16 >= have) break;
        if (base + 32 < have) fetch(base + 32, TA);
        __builtin_amdgcn_sched_barrier(0);
        compute(TB);
      }
      if (ovf_pos >= ovf_n) break;
      __builtin_amdgcn_wave_barrier();
      have = 0;
      while (ovf_pos < ovf_n && have <= CAP - 64 - 16) {
        const int i = ovf_pos + lane;
        const bool mine = (i < ovf_n) && (ovfg[i] == g);
        const unsigned long long bal = __ballot(mine);
        const int before = __popcll(bal & ((1ull << lane) - 1ull));
        if (mine) {
          const int4 r = ovf16[i];
          const int p = have + before;
          s_key[p] = r.x; s_src[p] = r.y; s_hxy[p] = r.z; s_hzf[p] = r.w; s_ri[p] = ngroups * CAP + i;
        }
        have += __popcll(bal);
        ovf_pos += 64;
      }
      if (lane < 16) {
        const int p = have + lane;
        s_key[p] = -1; s_src[p] = 0; s_hxy[p] = 0; s_hzf[p] = 0; s_ri[p] = 0;
      }
      __builtin_amdgcn_wave_barrier();
    }

    int ol = lane;
    asm volatile("" : "+v"(ol));
#pragma unroll
    for (int r = 0; r < 4; ++r) {
      const int node = g * GROUP + 4 * (ol >> 4) + r;
      if (node < N) {
#pragma unroll
        for (int c = 0; c < 4; ++c) {
          const size_t off = (size_t)node * 512 + 128 * c + 64 * hf + 4 * (ol & 15);
          const u32x2 x = *reinterpret_cast<const u32x2*>(sv + off);
          u32x2 o;
          o[0] = pack2(lo16(x[0]) + acc[c][0][r], hi16(x[0]) + acc[c][1][r]);
          o[1] = pack2(lo16(x[1]) + acc[c][2][r], hi16(x[1]) + acc[c][3][r]);
          *reinterpret_cast<u32x2*>(out + off) = o;
        }
      }
    }
    __builtin_amdgcn_wave_barrier();
  }
}

extern "C" void kernel_launch(void* const* d_in, const int* in_sizes, int n_in,
                              void* d_out, int out_size, void* d_ws, size_t ws_size,
                              hipStream_t stream) {
  const u16* sv = (const u16*)d_in[0];
  const u16* pos = (const u16*)d_in[1];
  const int* src = (const int*)d_in[2];
  const int* dst = (const int*)d_in[3];
  const u16* freqs = (const u16*)d_in[4];
  const u16* w1 = (const u16*)d_in[5];
  const u16* w2 = (const u16*)d_in[6];
  const u16* wr = (const u16*)d_in[7];
  const int N = in_sizes[0] / 512;
  const int E = in_sizes[2];
  const int ngroups = (N + GROUP - 1) / GROUP;
  const WsLayout L = ws_layout(N, ngroups, E);
  char* ws = (char*)d_ws;
  int* cnt = (int*)(ws + L.cnt);
  u16* w1f = (u16*)(ws + L.w1f);
  u16* wl = (u16*)(ws + L.wl);
  u32x2* pos4 = (u32x2*)(ws + L.pos4);
  u16* hbuf = (u16*)(ws + L.h);
  int4* rec16 = (int4*)(ws + L.rec16);
  u16* rbfa = (u16*)(ws + L.rbf);
  int4* ovf16 = (int4*)(ws + L.ovf16);
  int* ovfg = (int*)(ws + L.ovfg);

  int prep_n = (71680 > ngroups + 1) ? 71680 : ngroups + 1;
  if (N > prep_n) prep_n = N;
  k_prep<<<(prep_n + 255) / 256, 256, 0, stream>>>(w1, w2, wr, cnt, w1f, wl, pos, pos4, N, ngroups);
  const int h_blocks = (N + 127) / 128;
  const int edge_blocks = (E + 255) / 256;
  k_edges<<<h_blocks + edge_blocks, 256, 0, stream>>>(sv, pos4, src, dst, w1f, cnt, hbuf, freqs, rec16,
                                                       (u32x4*)rbfa, ovf16, ovfg, N, E, ngroups, h_blocks);
  const size_t lds = WL_BYTES + K2_WAVES * SCR_BYTES;
  static_assert(WL_BYTES + K2_WAVES * SCR_BYTES <= 163840, "lds");
  int grid = 256;
  if (ngroups < grid) grid = ngroups;
  k_message<<<grid, 512, lds, stream>>>(sv, cnt, wl, hbuf, rec16, rbfa, ovf16, ovfg, (u16*)d_out, N,
                                        ngroups);
}
```

```cpp
#include <hip/hip_runtime.h>
#include <hip/hip_bf16.h>
#include <stdint.h>

typedef unsigned short u16;
typedef unsigned int u32;
using bf16x8 = __attribute__((ext_vector_type(8))) short;
using bf16x4 = __attribute__((ext_vector_type(4))) short;
using f32x4 = __attribute__((ext_vector_type(4))) float;
using f32x2 = __attribute__((ext_vector_type(2))) float;
using u32x4 = __attribute__((ext_vector_type(4))) unsigned int;
using u32x2 = __attribute__((ext_vector_type(2))) unsigned int;

#define GROUP 16
#define CAP 128
#define CNT_STRIDE 32
#define W1_ELEMS 16384
#define W2_BYTES 98304
#define WR_BYTES 12288
#define WL_BYTES (W2_BYTES + WR_BYTES)
#define K2_WAVES 8
#define SCR_BYTES 2560

__device__ __forceinline__ float bf2f(u16 x) { return __uint_as_float(((u32)x) << 16); }
__device__ __forceinline__ u16 f2bf(float x) {
  __hip_bfloat16 b = __float2bfloat16(x);
  return *reinterpret_cast<u16*>(&b);
}
__device__ __forceinline__ float rb(float x) { return bf2f(f2bf(x)); }
typedef __bf16 hbf2 __attribute__((ext_vector_type(2)));
__device__ __forceinline__ u32 pack2(float lo, float hi) {
  f32x2 f = {lo, hi};
  hbf2 b = __builtin_convertvector(f, hbf2);
  return *reinterpret_cast<u32*>(&b);
}
__device__ __forceinline__ float lo16(u32 x) { return __uint_as_float(x << 16); }
__device__ __forceinline__ float hi16(u32 x) { return __uint_as_float(x & 0xffff0000u); }

struct WsLayout {
  size_t cnt, w1f, wl, pos4, h, rec16, rbf, ovf16, ovfg, total;
};
static inline size_t al256(size_t x) { return (x + 255) & ~(size_t)255; }
static inline WsLayout ws_layout(int N, int ngroups, int E) {
  WsLayout L;
  size_t o = 0;
  L.cnt = o;   o = al256(o + (size_t)(ngroups + 1) * CNT_STRIDE * 4);
  L.w1f = o;   o = al256(o + W1_ELEMS * 2);
  L.wl = o;    o = al256(o + WL_BYTES);
  L.pos4 = o;  o = al256(o + (size_t)N * 8);
  L.h = o;     o = al256(o + (size_t)N * 256);
  L.rec16 = o; o = al256(o + (size_t)ngroups * CAP * 16);
  L.rbf = o;   o = al256(o + ((size_t)ngroups * CAP + (size_t)E) * 32);
  L.ovf16 = o; o = al256(o + (size_t)E * 16);
  L.ovfg = o;  o = al256(o + (size_t)E * 4);
  L.total = o;
  return L;
}

__global__ __launch_bounds__(256) void k_prep(const u16* __restrict__ w1, const u16* __restrict__ w2,
                                              const u16* __restrict__ wr, int* __restrict__ cnt,
                                              u16* __restrict__ w1f, u16* __restrict__ wl,
                                              const u16* __restrict__ pos, u32x2* __restrict__ pos4,
                                              int N, int ngroups) {
  const int idx = (int)blockIdx.x * 256 + threadIdx.x;
  if (idx < 16384) {
    int j = idx & 7, lane = (idx >> 3) & 63, ks = (idx >> 9) & 3, t = idx >> 11;
    int q = lane >> 4, n = lane & 15;
    w1f[idx] = w1[(32 * ks + 8 * q + j) * 128 + 16 * t + n];
  } else if (idx < 16384 + 49152) {
    int i = idx - 16384;
    int j = i & 7, lane = (i >> 3) & 63, ks = (i >> 9) & 3, st = i >> 11;
    int hf = st / 12, tp = (st % 12) / 3, c = st % 3;
    int q = lane >> 4, n = lane & 15;
    int u = 16 * (2 * ks + (j >> 2)) + 4 * q + (j & 3);
    wl[i] = w2[u * 384 + 128 * c + 64 * hf + 4 * n + tp];
  } else if (idx < 16384 + 49152 + 6144) {
    int i = idx - 16384 - 49152;
    int j = i & 3, lane = (i >> 2) & 63, st = i >> 8;
    int hf = st / 12, tp = (st % 12) / 3, c = st % 3;
    int q = lane >> 4, n = lane & 15;
    wl[49152 + i] = wr[(4 * q + j) * 384 + 128 * c + 64 * hf + 4 * n + tp];
  }
  if (idx <= ngroups) cnt[idx * CNT_STRIDE] = 0;
  if (idx < N) {
    u32x2 p;
    p[0] = (u32)pos[idx * 3] | ((u32)pos[idx * 3 + 1] << 16);
    p[1] = (u32)pos[idx * 3 + 2];
    pos4[idx] = p;
  }
}

__global__ __launch_bounds__(256) void k_edges(
    const u16* __restrict__ sv, const u32x2* __restrict__ pos4, const int* __restrict__ src,
    const int* __restrict__ dst, const u16* __restrict__ w1f, int* __restrict__ cnt,
    u16* __restrict__ hbuf, const u16* __restrict__ freqs, int4* __restrict__ rec16,
    u32x4* __restrict__ rbfo, int4* __restrict__ ovf16, int* __restrict__ ovfg, int N, int E,
    int ngroups, int h_blocks) {
  __shared__ __attribute__((aligned(16))) u32x4 w1s[2048];
  if ((int)blockIdx.x < h_blocks) {
    const int lane = threadIdx.x & 63, wave = threadIdx.x >> 6;
    const int q = lane >> 4, n = lane & 15;
    const int node0 = ((int)blockIdx.x * 4 + wave) * 32;
    {
      const u32x4* g4 = reinterpret_cast<const u32x4*>(w1f);
      u32x4 tmp[8];
#pragma unroll
      for (int k = 0; k < 8; ++k) tmp[k] = g4[threadIdx.x + k * 256];
      bf16x8 sB[2][4];
#pragma unroll
      for (int u = 0; u < 2; ++u) {
        int node = node0 + 16 * u + n;
        if (node >= N) node = N - 1;
        const bf16x8* srow = reinterpret_cast<const bf16x8*>(sv + (size_t)node * 512);
#pragma unroll
        for (int ks = 0; ks < 4; ++ks) sB[u][ks] = srow[4 * ks + q];
      }
      __builtin_amdgcn_sched_barrier(0);
#pragma unroll
      for (int k = 0; k < 8; ++k) w1s[threadIdx.x + k * 256] = tmp[k];
      __syncthreads();
      const bf16x8* W1f = reinterpret_cast<const bf16x8*>(w1s);
      u32x4 hp[2][4];
#pragma unroll
      for (int t = 0; t < 8; ++t) {
        f32x4 d1[2] = {{0.f, 0.f, 0.f, 0.f}, {0.f, 0.f, 0.f, 0.f}};
#pragma unroll
        for (int ks = 0; ks < 4; ++ks) {
          const bf16x8 wfr = W1f[(t * 4 + ks) * 64 + lane];
          d1[0] = __builtin_amdgcn_mfma_f32_16x16x32_bf16(wfr, sB[0][ks], d1[0], 0, 0, 0);
          d1[1] = __builtin_amdgcn_mfma_f32_16x16x32_bf16(wfr, sB[1][ks], d1[1], 0, 0, 0);
        }
#pragma unroll
        for (int u = 0; u < 2; ++u) {
          float sl[4];
#pragma unroll
          for (int r = 0; r < 4; ++r) {
            const float a = rb(d1[u][r]);
            sl[r] = a * __builtin_amdgcn_rcpf(1.0f + __expf(-a));
          }
          hp[u][t >> 1][(t & 1) * 2 + 0] = pack2(sl[0], sl[1]);
          hp[u][t >> 1][(t & 1) * 2 + 1] = pack2(sl[2], sl[3]);
        }
      }
#pragma unroll
      for (int u = 0; u < 2; ++u) {
        const int node = node0 + 16 * u + n;
        if (node < N) {
          u32x4* hrow = reinterpret_cast<u32x4*>(hbuf + (size_t)node * 128);
#pragma unroll
          for (int ks = 0; ks < 4; ++ks) hrow[4 * ks + q] = hp[u][ks];
        }
      }
    }
    return;
  }
  u32x4* lst = w1s;
  int* lcnt = reinterpret_cast<int*>(w1s + 1024);
  if (threadIdx.x == 0) *lcnt = 0;
  __syncthreads();
  const int e0 = ((int)blockIdx.x - h_blocks) * 512 + threadIdx.x;
  int ee[2] = {e0, e0 + 256}, ss[2], dv[2];
#pragma unroll
  for (int u = 0; u < 2; ++u) {
    const bool ok = ee[u] < E;
    ss[u] = ok ? src[ee[u]] : 0;
    dv[u] = ok ? dst[ee[u]] : 0;
  }
  u32x2 ps[2], pd[2];
#pragma unroll
  for (int u = 0; u < 2; ++u) { ps[u] = pos4[ss[u]]; pd[u] = pos4[dv[u]]; }
#pragma unroll
  for (int u = 0; u < 2; ++u) {
    const float rx = rb(lo16(ps[u][0]) - lo16(pd[u][0]));
    const float ry = rb(hi16(ps[u][0]) - hi16(pd[u][0]));
    const float rz = rb(lo16(ps[u][1]) - lo16(pd[u][1]));
    float q2 = rb(rb(rx * rx) + rb(ry * ry) + rb(rz * rz));
    q2 = rb(q2 + 1e-8f);
    const float dd = rb(sqrtf(q2));
    if (ee[u] < E && dd < 5.0f) {
      const int p = atomicAdd(lcnt, 1);
      u32x4 a;
      a[0] = (u32)ee[u]; a[1] = (u32)ss[u]; a[2] = (u32)dv[u];
      a[3] = pack2(rx, ry);
      lst[2 * p] = a;
      lst[2 * p + 1][0] = pack2(rz, dd);
    }
  }
  __syncthreads();
  const int nact = *lcnt;
  for (int p = threadIdx.x; p < nact; p += 256) {
    const u32x4 a = lst[2 * p];
    const u32 zd = lst[2 * p + 1][0];
    const int e = (int)a[0], s = (int)a[1], d = (int)a[2];
    const float rx = lo16(a[3]), ry = hi16(a[3]), rz = lo16(zd), dd = hi16(zd);
    const int g = d >> 4;
    const int slot = atomicAdd(&cnt[g * CNT_STRIDE], 1);
    const float hx = rb(rx / dd), hy = rb(ry / dd), hz = rb(rz / dd);
    const float fc = rb(0.5f * rb(rb(cosf(rb(rb(3.141592653589793f * dd) / 5.0f))) + 1.0f));
    int4 r;
    r.x = (e << 4) | (d & 15);
    r.y = s;
    r.z = (int)pack2(hx, hy);
    r.w = (int)pack2(hz, fc);
    u32x4 rq[2];
#pragma unroll
    for (int k = 0; k < 8; ++k) {
      const u32 fq = *reinterpret_cast<const u32*>(freqs + 2 * k);
      const float s0 = rb(__sinf(rb(dd * lo16(fq))));
      const float s1 = rb(__sinf(rb(dd * hi16(fq))));
      rq[k >> 2][k & 3] = pack2(s0 / dd, s1 / dd);
    }
    size_t ridx;
    if (slot < CAP) {
      ridx = (size_t)g * CAP + slot;
      rec16[ridx] = r;
    } else {
      const int o = atomicAdd(&cnt[ngroups * CNT_STRIDE], 1);
      ovf16[o] = r;
      ovfg[o] = g;
      ridx = (size_t)ngroups * CAP + o;
    }
    rbfo[ridx * 2] = rq[0];
    rbfo[ridx * 2 + 1] = rq[1];
  }
}

__device__ __forceinline__ float bfx(const bf16x8& v, int i) {
  return __uint_as_float(((u32)(u16)v[i]) << 16);
}
__device__ __forceinline__ bf16x4 pack4(float a, float b, float c, float d) {
  u32x2 p;
  p[0] = pack2(a, b);
  p[1] = pack2(c, d);
  return *reinterpret_cast<bf16x4*>(&p);
}

__global__ __launch_bounds__(512) void k_message(
    const u16* __restrict__ sv, const int* __restrict__ cnt, const u16* __restrict__ wl_g,
    const u16* __restrict__ hbuf, const int4* __restrict__ rec16, const u16* __restrict__ rbfa,
    const int4* __restrict__ ovf16, const int* __restrict__ ovfg, u16* __restrict__ out, int N,
    int ngroups) {
  extern __shared__ __attribute__((aligned(16))) char smem[];
  const int tid = threadIdx.x;
  const int lane = tid & 63, wave = tid >> 6;

  {
    const u32x4* g4 = reinterpret_cast<const u32x4*>(wl_g);
    u32x4* l4 = reinterpret_cast<u32x4*>(smem);
    constexpr int NST = WL_BYTES / 16;
    constexpr int NIT = (NST + 511) / 512;
    u32x4 tmp[NIT];
#pragma unroll
    for (int k = 0; k < NIT; ++k) {
      const int i = tid + k * 512;
      if (i < NST) tmp[k] = g4[i];
    }
    __builtin_amdgcn_sched_barrier(0);
#pragma unroll
    for (int k = 0; k < NIT; ++k) {
      const int i = tid + k * 512;
      if (i < NST) l4[i] = tmp[k];
    }
  }
  const bf16x8* W2f = reinterpret_cast<const bf16x8*>(smem);
  const bf16x4* Wrf = reinterpret_cast<const bf16x4*>(smem + W2_BYTES);
  int* scr = reinterpret_cast<int*>(smem + WL_BYTES + wave * SCR_BYTES);
  int* s_key = scr;
  int* s_src = scr + 128;
  int* s_hxy = scr + 256;
  int* s_hzf = scr + 384;
  int* s_ri = scr + 512;

  __syncthreads();

  const char* svb = reinterpret_cast<const char*>(sv);
  const char* hb = reinterpret_cast<const char*>(hbuf);
  const char* rbb = reinterpret_cast<const char*>(rbfa);

  for (int k = wave; (int)blockIdx.x + (int)gridDim.x * (k >> 1) < ngroups; k += K2_WAVES) {
    const int g = (int)blockIdx.x + (int)gridDim.x * (k >> 1);
    const int hf = k & 1;
    const int total = cnt[g * CNT_STRIDE];
    const int nb = total < CAP ? total : CAP;
    const int ovf_n = (total > CAP) ? cnt[ngroups * CNT_STRIDE] : 0;

    {
      int bl = lane;
      asm volatile("" : "+v"(bl));
      int4 r0 = rec16[(size_t)g * CAP + bl], r1 = rec16[(size_t)g * CAP + bl + 64];
      if (bl >= nb) r0.x = 0x7fffffff;
      if (bl + 64 >= nb) r1.x = 0x7fffffff;
      s_key[bl] = r0.x;
      s_key[bl + 64] = r1.x;
      __builtin_amdgcn_wave_barrier();
      int k0 = 0, k1 = 64;
      for (int i = 0; i < nb; i += 4) {
        int4 kk = *reinterpret_cast<const int4*>(s_key + i);
        k0 += (kk.x < r0.x) + (kk.y < r0.x) + (kk.z < r0.x) + (kk.w < r0.x);
      }
      if (nb > 64) {
        k1 = 0;
        for (int i = 0; i < nb; i += 4) {
          int4 kk = *reinterpret_cast<const int4*>(s_key + i);
          k1 += (kk.x < r1.x) + (kk.y < r1.x) + (kk.z < r1.x) + (kk.w < r1.x);
        }
      }
      if (bl >= nb) { k0 = bl; r0 = make_int4(-1, 0, 0, 0); }
      if (bl + 64 >= nb) { k1 = bl + 64; r1 = make_int4(-1, 0, 0, 0); }
      __builtin_amdgcn_wave_barrier();
      s_key[k0] = r0.x; s_src[k0] = r0.y; s_hxy[k0] = r0.z; s_hzf[k0] = r0.w; s_ri[k0] = g * CAP + (bl < nb ? bl : 0);
      s_key[k1] = r1.x; s_src[k1] = r1.y; s_hxy[k1] = r1.z; s_hzf[k1] = r1.w; s_ri[k1] = g * CAP + (bl + 64 < nb ? bl + 64 : 0);
      __builtin_amdgcn_wave_barrier();
    }

    f32x4 acc[4][4];
#pragma unroll
    for (int c = 0; c < 4; ++c)
#pragma unroll
      for (int t = 0; t < 4; ++t) acc[c][t] = (f32x4){0.f, 0.f, 0.f, 0.f};

    int have = nb;
    int ovf_pos = 0;

    struct Tile {
      bf16x8 hA[4];
      u32x2 vr[4][3];
      bf16x4 rbfA, selA;
      int4 hxy4;
      u32 hz01, hz23;
    };
    auto fetch = [&](int base, Tile& T) {
      int fl = lane;
      asm volatile("" : "+v"(fl));
      const int q_ = fl >> 4, n_ = fl & 15;
      const u32 src_e = (u32)s_src[base + n_];
      const u32 ri_e = (u32)s_ri[base + n_];
      const int4 src4 = *reinterpret_cast<const int4*>(s_src + base + 4 * q_);
      const int4 key4 = *reinterpret_cast<const int4*>(s_key + base + 4 * q_);
      T.hxy4 = *reinterpret_cast<const int4*>(s_hxy + base + 4 * q_);
      const int4 hzf4 = *reinterpret_cast<const int4*>(s_hzf + base + 4 * q_);
      {
        const int kk[4] = {key4.x, key4.y, key4.z, key4.w};
        const u32 zz[4] = {(u32)hzf4.x, (u32)hzf4.y, (u32)hzf4.z, (u32)hzf4.w};
#pragma unroll
        for (int j = 0; j < 4; ++j)
          T.selA[j] = (kk[j] >= 0 && (kk[j] & 15) == n_) ? (short)(zz[j] >> 16) : (short)0;
        T.hz01 = (zz[0] & 0xffffu) | (zz[1] << 16);
        T.hz23 = (zz[2] & 0xffffu) | (zz[3] << 16);
      }
      const u32 ho = src_e * 256u + (u32)q_ * 16u;
#pragma unroll
      for (int ks = 0; ks < 4; ++ks) T.hA[ks] = *reinterpret_cast<const bf16x8*>(hb + (ho + 64u * ks));
      T.rbfA = *reinterpret_cast<const bf16x4*>(rbb + ((size_t)ri_e * 32u + (u32)q_ * 8u));
      const u32 vo = 256u + 128u * (u32)hf + 8u * (u32)n_;
      const u32 s4[4] = {(u32)src4.x, (u32)src4.y, (u32)src4.z, (u32)src4.w};
#pragma unroll
      for (int r = 0; r < 4; ++r) {
        const u32 o = s4[r] * 1024u + vo;
#pragma unroll
        for (int x = 0; x < 3; ++x) T.vr[r][x] = *reinterpret_cast<const u32x2*>(svb + (o + 256u * x));
      }
    };
    auto compute = [&](const Tile& T) {
      int wl = lane;
      asm volatile("" : "+v"(wl));
      const bf16x8* W2h = W2f + (size_t)hf * 12 * 4 * 64 + wl;
      const bf16x4* Wrh = Wrf + (size_t)hf * 12 * 64 + wl;
      bf16x8 wb[2][4];
      bf16x4 wrb[2];
#pragma unroll
      for (int ks = 0; ks < 4; ++ks) wb[0][ks] = W2h[ks * 64];
      wrb[0] = Wrh[0];
      const u32 hxy4[4] = {(u32)T.hxy4.x, (u32)T.hxy4.y, (u32)T.hxy4.z, (u32)T.hxy4.w};
      const bf16x4 selA = T.selA;
      float hat[3][4];
#pragma unroll
      for (int r = 0; r < 4; ++r) { hat[0][r] = lo16(hxy4[r]); hat[1][r] = hi16(hxy4[r]); }
      hat[2][0] = lo16(T.hz01); hat[2][1] = hi16(T.hz01);
      hat[2][2] = lo16(T.hz23); hat[2][3] = hi16(T.hz23);
      __builtin_amdgcn_sched_barrier(0);
      f32x4 m[3];
      f32x4 d2p, wfp;
#pragma unroll
      for (int st = 0; st <= 12; ++st) {
        const int cur = st & 1;
        if (st + 1 < 12) {
#pragma unroll
          for (int ks = 0; ks < 4; ++ks) wb[cur ^ 1][ks] = W2h[((st + 1) * 4 + ks) * 64];
          wrb[cur ^ 1] = Wrh[(st + 1) * 64];
        }
        __builtin_amdgcn_sched_barrier(0);
        f32x4 d2 = {0.f, 0.f, 0.f, 0.f};
        f32x4 wf = {0.f, 0.f, 0.f, 0.f};
        if (st < 12) {
          wf = __builtin_amdgcn_mfma_f32_16x16x16bf16_1k(T.rbfA, wrb[cur], wf, 0, 0, 0);
#pragma unroll
          for (int ks = 0; ks < 4; ++ks)
            d2 = __builtin_amdgcn_mfma_f32_16x16x32_bf16(T.hA[ks], wb[cur][ks], d2, 0, 0, 0);
        }
        __builtin_amdgcn_sched_barrier(0);
        if (st > 0) {
          const int pt = (st - 1) / 3, pc = (st - 1) % 3;
#pragma unroll
          for (int r = 0; r < 4; ++r) m[pc][r] = d2p[r] * wfp[r];
          if (pc == 2) {
            acc[0][pt] = __builtin_amdgcn_mfma_f32_16x16x16bf16_1k(
                selA, pack4(m[0][0], m[0][1], m[0][2], m[0][3]), acc[0][pt], 0, 0, 0);
#pragma unroll
            for (int x = 0; x < 3; ++x) {
              float mv[4];
#pragma unroll
              for (int r = 0; r < 4; ++r) {
                const u32 w = T.vr[r][x][pt >> 1];
                const float vv = (pt & 1) ? hi16(w) : lo16(w);
                mv[r] = m[1][r] * vv + m[2][r] * hat[x][r];
              }
              acc[1 + x][pt] = __builtin_amdgcn_mfma_f32_16x16x16bf16_1k(
                  selA, pack4(mv[0], mv[1], mv[2], mv[3]), acc[1 + x][pt], 0, 0, 0);
            }
          }
        }
        d2p = d2; wfp = wf;
        __builtin_amdgcn_sched_barrier(0);
      }
    };

    while (true) {
      Tile TA, TB;
      if (have > 0) fetch(0, TA);
      for (int base = 0; base < have; base += 32) {
        if (base + 16 < have) fetch(base + 16, TB);
        __builtin_amdgcn_sched_barrier(0);
        compute(TA);
        if (base + 16 >= have) break;
        if (base + 32 < have) fetch(base + 32, TA);
        __builtin_amdgcn_sched_barrier(0);
        compute(TB);
      }
      if (ovf_pos >= ovf_n) break;
      __builtin_amdgcn_wave_barrier();
      have = 0;
      while (ovf_pos < ovf_n && have <= CAP - 64 - 16) {
        const int i = ovf_pos + lane;
        const bool mine = (i < ovf_n) && (ovfg[i] == g);
        const unsigned long long bal = __ballot(mine);
        const int before = __popcll(bal & ((1ull << lane) - 1ull));
        if (mine) {
          const int4 r = ovf16[i];
          const int p = have + before;
          s_key[p] = r.x; s_src[p] = r.y; s_hxy[p] = r.z; s_hzf[p] = r.w; s_ri[p] = ngroups * CAP + i;
        }
        have += __popcll(bal);
        ovf_pos += 64;
      }
      if (lane < 16) {
        const int p = have + lane;
        s_key[p] = -1; s_src[p] = 0; s_hxy[p] = 0; s_hzf[p] = 0; s_ri[p] = 0;
      }
      __builtin_amdgcn_wave_barrier();
    }

    int ol = lane;
    asm volatile("" : "+v"(ol));
#pragma unroll
    for (int r = 0; r < 4; ++r) {
      const int node = g * GROUP + 4 * (ol >> 4) + r;
      if (node < N) {
#pragma unroll
        for (int c = 0; c < 4; ++c) {
          const size_t off = (size_t)node * 512 + 128 * c + 64 * hf + 4 * (ol & 15);
          const u32x2 x = *reinterpret_cast<const u32x2*>(sv + off);
          u32x2 o;
          o[0] = pack2(lo16(x[0]) + acc[c][0][r], hi16(x[0]) + acc[c][1][r]);
          o[1] = pack2(lo16(x[1]) + acc[c][2][r], hi16(x[1]) + acc[c][3][r]);
          *reinterpret_cast<u32x2*>(out + off) = o;
        }
      }
    }
    __builtin_amdgcn_wave_barrier();
  }
}

extern "C" void kernel_launch(void* const* d_in, const int* in_sizes, int n_in,
                              void* d_out, int out_size, void* d_ws, size_t ws_size,
                              hipStream_t stream) {
  const u16* sv = (const u16*)d_in[0];
  const u16* pos = (const u16*)d_in[1];
  const int* src = (const int*)d_in[2];
  const int* dst = (const int*)d_in[3];
  const u16* freqs = (const u16*)d_in[4];
  const u16* w1 = (const u16*)d_in[5];
  const u16* w2 = (const u16*)d_in[6];
  const u16* wr = (const u16*)d_in[7];
  const int N = in_sizes[0] / 512;
  const int E = in_sizes[2];
  const int ngroups = (N + GROUP - 1) / GROUP;
  const WsLayout L = ws_layout(N, ngroups, E);
  char* ws = (char*)d_ws;
  int* cnt = (int*)(ws + L.cnt);
  u16* w1f = (u16*)(ws + L.w1f);
  u16* wl = (u16*)(ws + L.wl);
  u32x2* pos4 = (u32x2*)(ws + L.pos4);
  u16* hbuf = (u16*)(ws + L.h);
  int4* rec16 = (int4*)(ws + L.rec16);
  u16* rbfa = (u16*)(ws + L.rbf);
  int4* ovf16 = (int4*)(ws + L.ovf16);
  int* ovfg = (int*)(ws + L.ovfg);

  int prep_n = (71680 > ngroups + 1) ? 71680 : ngroups + 1;
  if (N > prep_n) prep_n = N;
  k_prep<<<(prep_n + 255) / 256, 256, 0, stream>>>(w1, w2, wr, cnt, w1f, wl, pos, pos4, N, ngroups);
  const int h_blocks = (N + 127) / 128;
  const int edge_blocks = (E + 511) / 512;
  k_edges<<<h_blocks + edge_blocks, 256, 0, stream>>>(sv, pos4, src, dst, w1f, cnt, hbuf, freqs, rec16,
                                                       (u32x4*)rbfa, ovf16, ovfg, N, E, ngroups, h_blocks);
  const size_t lds = WL_BYTES + K2_WAVES * SCR_BYTES;
  static_assert(WL_BYTES + K2_WAVES * SCR_BYTES <= 163840, "lds");
  int grid = 256;
  if (ngroups < grid) grid = ngroups;
  k_message<<<grid, 512, lds, stream>>>(sv, cnt, wl, hbuf, rec16, rbfa, ovf16, ovfg, (u16*)d_out, N,
                                        ngroups);
}
```

```cpp
#include <hip/hip_runtime.h>
#include <hip/hip_bf16.h>
#include <stdint.h>

typedef unsigned short u16;
typedef unsigned int u32;
using bf16x8 = __attribute__((ext_vector_type(8))) short;
using bf16x4 = __attribute__((ext_vector_type(4))) short;
using f32x4 = __attribute__((ext_vector_type(4))) float;
using f32x2 = __attribute__((ext_vector_type(2))) float;
using u32x4 = __attribute__((ext_vector_type(4))) unsigned int;
using u32x2 = __attribute__((ext_vector_type(2))) unsigned int;

#define GROUP 16
#define CAP 128
#define CNT_STRIDE 32
#define W1_ELEMS 16384
#define W2_BYTES 98304
#define WR_BYTES 12288
#define WL_BYTES (W2_BYTES + WR_BYTES)
#define K2_WAVES 8
#define SCR_BYTES 2560

__device__ __forceinline__ float bf2f(u16 x) { return __uint_as_float(((u32)x) << 16); }
__device__ __forceinline__ u16 f2bf(float x) {
  __hip_bfloat16 b = __float2bfloat16(x);
  return *reinterpret_cast<u16*>(&b);
}
__device__ __forceinline__ float rb(float x) { return bf2f(f2bf(x)); }
typedef __bf16 hbf2 __attribute__((ext_vector_type(2)));
__device__ __forceinline__ u32 pack2(float lo, float hi) {
  f32x2 f = {lo, hi};
  hbf2 b = __builtin_convertvector(f, hbf2);
  return *reinterpret_cast<u32*>(&b);
}
__device__ __forceinline__ float lo16(u32 x) { return __uint_as_float(x << 16); }
__device__ __forceinline__ float hi16(u32 x) { return __uint_as_float(x & 0xffff0000u); }

struct WsLayout {
  size_t cnt, w1f, wl, pos4, h, rec16, rbf, ovf16, ovfg, total;
};
static inline size_t al256(size_t x) { return (x + 255) & ~(size_t)255; }
static inline WsLayout ws_layout(int N, int ngroups, int E) {
  WsLayout L;
  size_t o = 0;
  L.cnt = o;   o = al256(o + (size_t)(ngroups + 1) * CNT_STRIDE * 4);
  L.w1f = o;   o = al256(o + W1_ELEMS * 2);
  L.wl = o;    o = al256(o + WL_BYTES);
  L.pos4 = o;  o = al256(o + (size_t)N * 8);
  L.h = o;     o = al256(o + (size_t)N * 256);
  L.rec16 = o; o = al256(o + (size_t)ngroups * CAP * 16);
  L.rbf = o;   o = al256(o + ((size_t)ngroups * CAP + (size_t)E) * 32);
  L.ovf16 = o; o = al256(o + (size_t)E * 16);
  L.ovfg = o;  o = al256(o + (size_t)E * 4);
  L.total = o;
  return L;
}

__global__ __launch_bounds__(256) void k_prep(const u16* __restrict__ w1, const u16* __restrict__ w2,
                                              const u16* __restrict__ wr, int* __restrict__ cnt,
                                              u16* __restrict__ w1f, u16* __restrict__ wl,
                                              const u16* __restrict__ pos, u32x2* __restrict__ pos4,
                                              int N, int ngroups) {
  const int idx = (int)blockIdx.x * 256 + threadIdx.x;
  if (idx < 16384) {
    int j = idx & 7, lane = (idx >> 3) & 63, ks = (idx >> 9) & 3, t = idx >> 11;
    int q = lane >> 4, n = lane & 15;
    w1f[idx] = w1[(32 * ks + 8 * q + j) * 128 + 16 * t + n];
  } else if (idx < 16384 + 49152) {
    int i = idx - 16384;
    int j = i & 7, lane = (i >> 3) & 63, ks = (i >> 9) & 3, st = i >> 11;
    int w = st / 3, c = st % 3;
    int q = lane >> 4, n = lane & 15;
    int u = 16 * (2 * ks + (j >> 2)) + 4 * q + (j & 3);
    wl[i] = w2[u * 384 + 128 * c + 16 * w + n];
  } else if (idx < 16384 + 49152 + 6144) {
    int i = idx - 16384 - 49152;
    int j = i & 3, lane = (i >> 2) & 63, st = i >> 8;
    int w = st / 3, c = st % 3;
    int q = lane >> 4, n = lane & 15;
    wl[49152 + i] = wr[(4 * q + j) * 384 + 128 * c + 16 * w + n];
  }
  if (idx <= ngroups) cnt[idx * CNT_STRIDE] = 0;
  if (idx < N) {
    u32x2 p;
    p[0] = (u32)pos[idx * 3] | ((u32)pos[idx * 3 + 1] << 16);
    p[1] = (u32)pos[idx * 3 + 2];
    pos4[idx] = p;
  }
}

__global__ __launch_bounds__(256) void k_edges(
    const u16* __restrict__ sv, const u32x2* __restrict__ pos4, const int* __restrict__ src,
    const int* __restrict__ dst, const u16* __restrict__ w1f, int* __restrict__ cnt,
    u16* __restrict__ hbuf, const u16* __restrict__ freqs, int4* __restrict__ rec16,
    u32x4* __restrict__ rbfo, int4* __restrict__ ovf16, int* __restrict__ ovfg, int N, int E,
    int ngroups, int h_blocks) {
  __shared__ __attribute__((aligned(16))) u32x4 w1s[2048];
  if ((int)blockIdx.x < h_blocks) {
    const int lane = threadIdx.x & 63, wave = threadIdx.x >> 6;
    const int q = lane >> 4, n = lane & 15;
    const int node0 = ((int)blockIdx.x * 4 + wave) * 32;
    {
      const u32x4* g4 = reinterpret_cast<const u32x4*>(w1f);
      u32x4 tmp[8];
#pragma unroll
      for (int k = 0; k < 8; ++k) tmp[k] = g4[threadIdx.x + k * 256];
      bf16x8 sB[2][4];
#pragma unroll
      for (int u = 0; u < 2; ++u) {
        int node = node0 + 16 * u + n;
        if (node >= N) node = N - 1;
        const bf16x8* srow = reinterpret_cast<const bf16x8*>(sv + (size_t)node * 512);
#pragma unroll
        for (int ks = 0; ks < 4; ++ks) sB[u][ks] = srow[4 * ks + q];
      }
      __builtin_amdgcn_sched_barrier(0);
#pragma unroll
      for (int k = 0; k < 8; ++k) w1s[threadIdx.x + k * 256] = tmp[k];
      __syncthreads();
      const bf16x8* W1f = reinterpret_cast<const bf16x8*>(w1s);
      u32x4 hp[2][4];
#pragma unroll
      for (int t = 0; t < 8; ++t) {
        f32x4 d1[2] = {{0.f, 0.f, 0.f, 0.f}, {0.f, 0.f, 0.f, 0.f}};
#pragma unroll
        for (int ks = 0; ks < 4; ++ks) {
          const bf16x8 wfr = W1f[(t * 4 + ks) * 64 + lane];
          d1[0] = __builtin_amdgcn_mfma_f32_16x16x32_bf16(wfr, sB[0][ks], d1[0], 0, 0, 0);
          d1[1] = __builtin_amdgcn_mfma_f32_16x16x32_bf16(wfr, sB[1][ks], d1[1], 0, 0, 0);
        }
#pragma unroll
        for (int u = 0; u < 2; ++u) {
          float sl[4];
#pragma unroll
          for (int r = 0; r < 4; ++r) {
            const float a = rb(d1[u][r]);
            sl[r] = a * __builtin_amdgcn_rcpf(1.0f + __expf(-a));
          }
          hp[u][t >> 1][(t & 1) * 2 + 0] = pack2(sl[0], sl[1]);
          hp[u][t >> 1][(t & 1) * 2 + 1] = pack2(sl[2], sl[3]);
        }
      }
#pragma unroll
      for (int u = 0; u < 2; ++u) {
        const int node = node0 + 16 * u + n;
        if (node < N) {
          u32x4* hrow = reinterpret_cast<u32x4*>(hbuf + (size_t)node * 128);
#pragma unroll
          for (int ks = 0; ks < 4; ++ks) hrow[4 * ks + q] = hp[u][ks];
        }
      }
    }
    return;
  }
  u32x4* lst = w1s;
  int* lcnt = reinterpret_cast<int*>(w1s + 1024);
  if (threadIdx.x == 0) *lcnt = 0;
  __syncthreads();
  const int e0 = ((int)blockIdx.x - h_blocks) * 512 + threadIdx.x;
  int ee[2] = {e0, e0 + 256}, ss[2], dv[2];
#pragma unroll
  for (int u = 0; u < 2; ++u) {
    const bool ok = ee[u] < E;
    ss[u] = ok ? src[ee[u]] : 0;
    dv[u] = ok ? dst[ee[u]] : 0;
  }
  u32x2 ps[2], pd[2];
#pragma unroll
  for (int u = 0; u < 2; ++u) { ps[u] = pos4[ss[u]]; pd[u] = pos4[dv[u]]; }
#pragma unroll
  for (int u = 0; u < 2; ++u) {
    const float rx = rb(lo16(ps[u][0]) - lo16(pd[u][0]));
    const float ry = rb(hi16(ps[u][0]) - hi16(pd[u][0]));
    const float rz = rb(lo16(ps[u][1]) - lo16(pd[u][1]));
    float q2 = rb(rb(rx * rx) + rb(ry * ry) + rb(rz * rz));
    q2 = rb(q2 + 1e-8f);
    const float dd = rb(sqrtf(q2));
    if (ee[u] < E && dd < 5.0f) {
      const int p = atomicAdd(lcnt, 1);
      u32x4 a;
      a[0] = (u32)ee[u]; a[1] = (u32)ss[u]; a[2] = (u32)dv[u];
      a[3] = pack2(rx, ry);
      lst[2 * p] = a;
      lst[2 * p + 1][0] = pack2(rz, dd);
    }
  }
  __syncthreads();
  const int nact = *lcnt;
  for (int p = threadIdx.x; p < nact; p += 256) {
    const u32x4 a = lst[2 * p];
    const u32 zd = lst[2 * p + 1][0];
    const int e = (int)a[0], s = (int)a[1], d = (int)a[2];
    const float rx = lo16(a[3]), ry = hi16(a[3]), rz = lo16(zd), dd = hi16(zd);
    const int g = d >> 4;
    const int slot = atomicAdd(&cnt[g * CNT_STRIDE], 1);
    const float hx = rb(rx / dd), hy = rb(ry / dd), hz = rb(rz / dd);
    const float fc = rb(0.5f * rb(rb(cosf(rb(rb(3.141592653589793f * dd) / 5.0f))) + 1.0f));
    int4 r;
    r.x = (e << 4) | (d & 15);
    r.y = s;
    r.z = (int)pack2(hx, hy);
    r.w = (int)pack2(hz, fc);
    u32x4 rq[2];
#pragma unroll
    for (int k = 0; k < 8; ++k) {
      const u32 fq = *reinterpret_cast<const u32*>(freqs + 2 * k);
      const float s0 = rb(__sinf(rb(dd * lo16(fq))));
      const float s1 = rb(__sinf(rb(dd * hi16(fq))));
      rq[k >> 2][k & 3] = pack2(s0 / dd, s1 / dd);
    }
    size_t ridx;
    if (slot < CAP) {
      ridx = (size_t)g * CAP + slot;
      rec16[ridx] = r;
    } else {
      const int o = atomicAdd(&cnt[ngroups * CNT_STRIDE], 1);
      ovf16[o] = r;
      ovfg[o] = g;
      ridx = (size_t)ngroups * CAP + o;
    }
    rbfo[ridx * 2] = rq[0];
    rbfo[ridx * 2 + 1] = rq[1];
  }
}

__device__ __forceinline__ float bfx(const bf16x8& v, int i) {
  return __uint_as_float(((u32)(u16)v[i]) << 16);
}
__device__ __forceinline__ bf16x4 pack4(float a, float b, float c, float d) {
  u32x2 p;
  p[0] = pack2(a, b);
  p[1] = pack2(c, d);
  return *reinterpret_cast<bf16x4*>(&p);
}

#define RSLOTS 6
#define EDGE_STRIDE 1040
#define SLOT_RBF (16 * EDGE_STRIDE)
#define SLOT_BYTES (SLOT_RBF + 512)
#define L_REC (RSLOTS * SLOT_BYTES)
#define L_OBUF (L_REC + 8 * 5 * 128 * 4)
#define L_OWN (L_OBUF + 16384)
#define L_META (L_OWN + 16384)
#define K2_LDS (L_META + 64)

__device__ __forceinline__ void dma16(const void* src, void* lds_wave_base) {
  __builtin_amdgcn_global_load_lds((const unsigned*)src, (unsigned*)lds_wave_base, 16, 0, 0);
}
__device__ __forceinline__ void wait_vm(int n) {
  if (n >= 15) asm volatile("s_waitcnt vmcnt(15)" ::: "memory");
  else if (n >= 12) asm volatile("s_waitcnt vmcnt(12)" ::: "memory");
  else if (n >= 9) asm volatile("s_waitcnt vmcnt(9)" ::: "memory");
  else if (n >= 6) asm volatile("s_waitcnt vmcnt(6)" ::: "memory");
  else if (n >= 3) asm volatile("s_waitcnt vmcnt(3)" ::: "memory");
  else asm volatile("s_waitcnt vmcnt(0)" ::: "memory");
}
__device__ __forceinline__ void raw_barrier() {
  asm volatile("s_waitcnt lgkmcnt(0)" ::: "memory");
  __builtin_amdgcn_s_barrier();
  asm volatile("" ::: "memory");
}
__device__ __forceinline__ int uni(int x) { return __builtin_amdgcn_readfirstlane(x); }

__global__ __launch_bounds__(512) void k_message(
    const u16* __restrict__ sv, const int* __restrict__ cnt, const u16* __restrict__ wl_g,
    const u16* __restrict__ hbuf, const int4* __restrict__ rec16, const u16* __restrict__ rbfa,
    const int4* __restrict__ ovf16, const int* __restrict__ ovfg, u16* __restrict__ out, int N,
    int ngroups) {
  extern __shared__ __attribute__((aligned(16))) char smem[];
  const int tid = threadIdx.x;
  const int lane = tid & 63, wave = uni(tid >> 6);
  const int q = lane >> 4, n = lane & 15;
  const char* svb = reinterpret_cast<const char*>(sv);
  const char* hb = reinterpret_cast<const char*>(hbuf);
  const char* rbb = reinterpret_cast<const char*>(rbfa);
  int* rec = reinterpret_cast<int*>(smem + L_REC);
  int* meta = reinterpret_cast<int*>(smem + L_META);

  bf16x8 w2[3][4];
  bf16x4 wr[3];
  {
    const bf16x8* W2g = reinterpret_cast<const bf16x8*>(wl_g);
    const bf16x4* Wrg = reinterpret_cast<const bf16x4*>(wl_g + 49152);
#pragma unroll
    for (int c = 0; c < 3; ++c) {
#pragma unroll
      for (int ks = 0; ks < 4; ++ks) w2[c][ks] = W2g[((wave * 3 + c) * 4 + ks) * 64 + lane];
      wr[c] = Wrg[(wave * 3 + c) * 64 + lane];
    }
  }

  auto run = [&](int nit, int gbase, int gstep, const char* ownsrc) {
    int pi = 0, pb = 0, issued = 0, t = 0;
    while (pi < nit && uni(meta[pi]) == 0) ++pi;
    auto issue = [&]() {
      char* S = smem + (issued % RSLOTS) * SLOT_BYTES;
      const int* R = rec + pi * 640;
#pragma unroll
      for (int u = 0; u < 2; ++u) {
        const int e = 2 * wave + u;
        const u32 se = (u32)uni(R[128 + pb + e]);
        const char* src = (lane < 16) ? hb + (se * 256u + (u32)lane * 16u)
                                      : svb + (se * 1024u + 256u + (u32)(lane - 16) * 16u);
        dma16(src, S + e * EDGE_STRIDE);
      }
      if (lane < 4) {
        const u32 ri = (u32)R[512 + pb + 2 * wave + (lane >> 1)];
        dma16(rbb + ((size_t)ri * 32u + (u32)(lane & 1) * 16u), S + SLOT_RBF + wave * 64);
      }
      ++issued;
      pb += 16;
      while (pi < nit && pb >= uni(meta[pi])) { ++pi; pb = 0; }
    };
    for (int k = 0; k < RSLOTS - 1; ++k)
      if (pi < nit) issue();

    for (int i = 0; i < nit; ++i) {
      const int g = gbase + i * gstep;
      const int ci = uni(meta[i]);
      {
        char* O = smem + L_OWN;
#pragma unroll
        for (int u = 0; u < 2; ++u) {
          int node = g * GROUP + 2 * wave + u;
          if (node >= N) node = N - 1;
          dma16(ownsrc + ((u32)node * 1024u + (u32)lane * 16u), O + (2 * wave + u) * 1024);
        }
      }
      const int issued0 = issued;
      f32x4 acc[4];
#pragma unroll
      for (int c = 0; c < 4; ++c) acc[c] = (f32x4){0.f, 0.f, 0.f, 0.f};
      const int* R = rec + i * 640;

      for (int base = 0; base < ci; base += 16) {
        wait_vm(3 * (issued - t - 1));
        raw_barrier();
        if (pi < nit) issue();
        const char* S = smem + (t % RSLOTS) * SLOT_BYTES;
        ++t;
        bf16x8 hA[4];
#pragma unroll
        for (int ks = 0; ks < 4; ++ks)
          hA[ks] = *reinterpret_cast<const bf16x8*>(S + n * EDGE_STRIDE + ks * 64 + q * 16);
        const bf16x4 rbfA = *reinterpret_cast<const bf16x4*>(S + SLOT_RBF + n * 32 + q * 8);
        const int4 key4 = *reinterpret_cast<const int4*>(R + base + 4 * q);
        const int4 hxy4 = *reinterpret_cast<const int4*>(R + 256 + base + 4 * q);
        const int4 hzf4 = *reinterpret_cast<const int4*>(R + 384 + base + 4 * q);
        float vv[3][4];
#pragma unroll
        for (int r = 0; r < 4; ++r)
#pragma unroll
          for (int x = 0; x < 3; ++x)
            vv[x][r] = bf2f(*reinterpret_cast<const u16*>(S + (4 * q + r) * EDGE_STRIDE + 256 + x * 256 +
                                                          (16 * wave + n) * 2));
        const int kk[4] = {key4.x, key4.y, key4.z, key4.w};
        const u32 xy[4] = {(u32)hxy4.x, (u32)hxy4.y, (u32)hxy4.z, (u32)hxy4.w};
        const u32 zf[4] = {(u32)hzf4.x, (u32)hzf4.y, (u32)hzf4.z, (u32)hzf4.w};
        bf16x4 selA;
#pragma unroll
        for (int j = 0; j < 4; ++j)
          selA[j] = (kk[j] >= 0 && (kk[j] & 15) == n) ? (short)(zf[j] >> 16) : (short)0;
        f32x4 m[3];
#pragma unroll
        for (int c = 0; c < 3; ++c) {
          f32x4 wf = {0.f, 0.f, 0.f, 0.f};
          wf = __builtin_amdgcn_mfma_f32_16x16x16bf16_1k(rbfA, wr[c], wf, 0, 0, 0);
          f32x4 d2 = {0.f, 0.f, 0.f, 0.f};
#pragma unroll
          for (int ks = 0; ks < 4; ++ks)
            d2 = __builtin_amdgcn_mfma_f32_16x16x32_bf16(hA[ks], w2[c][ks], d2, 0, 0, 0);
#pragma unroll
          for (int r = 0; r < 4; ++r) m[c][r] = d2[r] * wf[r];
        }
        acc[0] = __builtin_amdgcn_mfma_f32_16x16x16bf16_1k(
            selA, pack4(m[0][0], m[0][1], m[0][2], m[0][3]), acc[0], 0, 0, 0);
#pragma unroll
        for (int x = 0; x < 3; ++x) {
          float mv[4];
#pragma unroll
          for (int r = 0; r < 4; ++r) {
            const float hh = (x == 0) ? lo16(xy[r]) : ((x == 1) ? hi16(xy[r]) : lo16(zf[r]));
            mv[r] = m[1][r] * vv[x][r] + m[2][r] * hh;
          }
          acc[1 + x] = __builtin_amdgcn_mfma_f32_16x16x16bf16_1k(
              selA, pack4(mv[0], mv[1], mv[2], mv[3]), acc[1 + x], 0, 0, 0);
        }
      }
      {
        u16* ob = reinterpret_cast<u16*>(smem + L_OBUF);
#pragma unroll
        for (int c = 0; c < 4; ++c)
#pragma unroll
          for (int r = 0; r < 4; ++r) ob[(4 * q + r) * 512 + c * 128 + 16 * wave + n] = f2bf(acc[c][r]);
        wait_vm(3 * (issued - issued0));
        raw_barrier();
        const int node = g * GROUP + (tid >> 5);
        const u32x4* o4 = reinterpret_cast<const u32x4*>(smem + L_OBUF + tid * 32);
        const u32x4* w4 = reinterpret_cast<const u32x4*>(smem + L_OWN + tid * 32);
        u32x4 res[2];
#pragma unroll
        for (int h = 0; h < 2; ++h) {
          const u32x4 dl = o4[h], ow = w4[h];
#pragma unroll
          for (int k = 0; k < 4; ++k)
            res[h][k] = pack2(lo16(ow[k]) + lo16(dl[k]), hi16(ow[k]) + hi16(dl[k]));
        }
        if (node < N) {
          u32x4* dst = reinterpret_cast<u32x4*>(reinterpret_cast<char*>(out) + ((size_t)g * GROUP * 1024 + tid * 32));
          dst[0] = res[0];
          dst[1] = res[1];
        }
        raw_barrier();
      }
    }
    asm volatile("s_waitcnt vmcnt(0)" ::: "memory");
  };

  for (int b0 = (int)blockIdx.x; b0 < ngroups; b0 += (int)gridDim.x * 8) {
    const int gi = b0 + (int)gridDim.x * wave;
    int nit = (ngroups - b0 + (int)gridDim.x - 1) / (int)gridDim.x;
    if (nit > 8) nit = 8;
    if (gi < ngroups) {
      int* R = rec + wave * 640;
      const int total = cnt[gi * CNT_STRIDE];
      int4 r0 = rec16[(size_t)gi * CAP + lane], r1 = rec16[(size_t)gi * CAP + lane + 64];
      const int nb = total < CAP ? total : CAP;
      if (lane >= nb) r0.x = 0x7fffffff;
      if (lane + 64 >= nb) r1.x = 0x7fffffff;
      R[lane] = r0.x;
      R[lane + 64] = r1.x;
      __builtin_amdgcn_wave_barrier();
      int k0 = 0, k1 = 64;
      for (int i = 0; i < nb; i += 4) {
        const int4 kk = *reinterpret_cast<const int4*>(R + i);
        k0 += (kk.x < r0.x) + (kk.y < r0.x) + (kk.z < r0.x) + (kk.w < r0.x);
      }
      if (nb > 64) {
        k1 = 0;
        for (int i = 0; i < nb; i += 4) {
          const int4 kk = *reinterpret_cast<const int4*>(R + i);
          k1 += (kk.x < r1.x) + (kk.y < r1.x) + (kk.z < r1.x) + (kk.w < r1.x);
        }
      }
      if (lane >= nb) { k0 = lane; r0 = make_int4(-1, 0, 0, 0); }
      if (lane + 64 >= nb) { k1 = lane + 64; r1 = make_int4(-1, 0, 0, 0); }
      __builtin_amdgcn_wave_barrier();
      R[k0] = r0.x; R[128 + k0] = r0.y; R[256 + k0] = r0.z; R[384 + k0] = r0.w;
      R[512 + k0] = gi * CAP + (lane < nb ? lane : 0);
      R[k1] = r1.x; R[128 + k1] = r1.y; R[256 + k1] = r1.z; R[384 + k1] = r1.w;
      R[512 + k1] = gi * CAP + (lane + 64 < nb ? lane + 64 : 0);
      if (lane == 0) { meta[wave] = nb; meta[8 + wave] = total; }
    } else if (lane == 0) {
      meta[wave] = 0; meta[8 + wave] = 0;
    }
    __syncthreads();
    run(nit, b0, (int)gridDim.x, svb);

    int tot[8];
#pragma unroll
    for (int i = 0; i < 8; ++i) tot[i] = uni(meta[8 + i]);
    __syncthreads();
#pragma unroll 1
    for (int i = 0; i < nit; ++i) {
      int ti = 0;
#pragma unroll
      for (int j = 0; j < 8; ++j) ti = (i == j) ? tot[j] : ti;
      if (ti <= CAP) continue;
      const int g = b0 + i * (int)gridDim.x;
      const int ovf_n = cnt[ngroups * CNT_STRIDE];
      int pos_ = 0;
      while (pos_ < ovf_n) {
        if (wave == 0) {
          int have = 0;
          while (pos_ < ovf_n && have <= CAP - 64 - 16) {
            const int j = pos_ + lane;
            const bool mine = (j < ovf_n) && (ovfg[j] == g);
            const unsigned long long bal = __ballot(mine);
            const int before = __popcll(bal & ((1ull << lane) - 1ull));
            if (mine) {
              const int4 r = ovf16[j];
              const int p = have + before;
              rec[p] = r.x; rec[128 + p] = r.y; rec[256 + p] = r.z; rec[384 + p] = r.w;
              rec[512 + p] = ngroups * CAP + j;
            }
            have += __popcll(bal);
            pos_ += 64;
          }
          if (lane < 16) {
            const int p = have + lane;
            rec[p] = -1; rec[128 + p] = 0; rec[256 + p] = 0; rec[384 + p] = 0; rec[512 + p] = 0;
          }
          if (lane == 0) { meta[0] = have; meta[1] = pos_; }
        }
        __syncthreads();
        pos_ = uni(meta[1]);
        const int have = uni(meta[0]);
        __threadfence();
        if (have > 0) run(1, g, 0, reinterpret_cast<const char*>(out));
        __syncthreads();
      }
    }
    __syncthreads();
  }
}

extern "C" void kernel_launch(void* const* d_in, const int* in_sizes, int n_in,
                              void* d_out, int out_size, void* d_ws, size_t ws_size,
                              hipStream_t stream) {
  const u16* sv = (const u16*)d_in[0];
  const u16* pos = (const u16*)d_in[1];
  const int* src = (const int*)d_in[2];
  const int* dst = (const int*)d_in[3];
  const u16* freqs = (const u16*)d_in[4];
  const u16* w1 = (const u16*)d_in[5];
  const u16* w2 = (const u16*)d_in[6];
  const u16* wr = (const u16*)d_in[7];
  const int N = in_sizes[0] / 512;
  const int E = in_sizes[2];
  const int ngroups = (N + GROUP - 1) / GROUP;
  const WsLayout L = ws_layout(N, ngroups, E);
  char* ws = (char*)d_ws;
  int* cnt = (int*)(ws + L.cnt);
  u16* w1f = (u16*)(ws + L.w1f);
  u16* wl = (u16*)(ws + L.wl);
  u32x2* pos4 = (u32x2*)(ws + L.pos4);
  u16* hbuf = (u16*)(ws + L.h);
  int4* rec16 = (int4*)(ws + L.rec16);
  u16* rbfa = (u16*)(ws + L.rbf);
  int4* ovf16 = (int4*)(ws + L.ovf16);
  int* ovfg = (int*)(ws + L.ovfg);

  int prep_n = (71680 > ngroups + 1) ? 71680 : ngroups + 1;
  if (N > prep_n) prep_n = N;
  k_prep<<<(prep_n + 255) / 256, 256, 0, stream>>>(w1, w2, wr, cnt, w1f, wl, pos, pos4, N, ngroups);
  const int h_blocks = (N + 127) / 128;
  const int edge_blocks = (E + 511) / 512;
  k_edges<<<h_blocks + edge_blocks, 256, 0, stream>>>(sv, pos4, src, dst, w1f, cnt, hbuf, freqs, rec16,
                                                       (u32x4*)rbfa, ovf16, ovfg, N, E, ngroups, h_blocks);
  const size_t lds = K2_LDS;
  static_assert(K2_LDS <= 163840, "lds");
  int grid = 256;
  if (ngroups < grid) grid = ngroups;
  k_message<<<grid, 512, lds, stream>>>(sv, cnt, wl, hbuf, rec16, rbfa, ovf16, ovfg, (u16*)d_out, N,
                                        ngroups);
}
```

```cpp
#include <hip/hip_runtime.h>
#include <hip/hip_bf16.h>
#include <stdint.h>

typedef unsigned short u16;
typedef unsigned int u32;
using bf16x8 = __attribute__((ext_vector_type(8))) short;
using bf16x4 = __attribute__((ext_vector_type(4))) short;
using f32x4 = __attribute__((ext_vector_type(4))) float;
using f32x2 = __attribute__((ext_vector_type(2))) float;
using u32x4 = __attribute__((ext_vector_type(4))) unsigned int;
using u32x2 = __attribute__((ext_vector_type(2))) unsigned int;

#define GROUP 16
#define CAP 128
#define CNT_STRIDE 32
#define W1_ELEMS 16384
#define W2_BYTES 98304
#define WR_BYTES 12288
#define WL_BYTES (W2_BYTES + WR_BYTES)
#define K2_WAVES 8
#define SCR_BYTES 2560

__device__ __forceinline__ float bf2f(u16 x) { return __uint_as_float(((u32)x) << 16); }
__device__ __forceinline__ u16 f2bf(float x) {
  __hip_bfloat16 b = __float2bfloat16(x);
  return *reinterpret_cast<u16*>(&b);
}
__device__ __forceinline__ float rb(float x) { return bf2f(f2bf(x)); }
typedef __bf16 hbf2 __attribute__((ext_vector_type(2)));
__device__ __forceinline__ u32 pack2(float lo, float hi) {
  f32x2 f = {lo, hi};
  hbf2 b = __builtin_convertvector(f, hbf2);
  return *reinterpret_cast<u32*>(&b);
}
__device__ __forceinline__ float lo16(u32 x) { return __uint_as_float(x << 16); }
__device__ __forceinline__ float hi16(u32 x) { return __uint_as_float(x & 0xffff0000u); }

struct WsLayout {
  size_t cnt, w1f, wl, pos4, h, rec16, rbf, ovf16, ovfg, total;
};
static inline size_t al256(size_t x) { return (x + 255) & ~(size_t)255; }
static inline WsLayout ws_layout(int N, int ngroups, int E) {
  WsLayout L;
  size_t o = 0;
  L.cnt = o;   o = al256(o + (size_t)(ngroups + 1) * CNT_STRIDE * 4);
  L.w1f = o;   o = al256(o + W1_ELEMS * 2);
  L.wl = o;    o = al256(o + WL_BYTES);
  L.pos4 = o;  o = al256(o + (size_t)N * 8);
  L.h = o;     o = al256(o + (size_t)N * 256);
  L.rec16 = o; o = al256(o + (size_t)ngroups * CAP * 16);
  L.rbf = o;   o = al256(o + ((size_t)ngroups * CAP + (size_t)E) * 32);
  L.ovf16 = o; o = al256(o + (size_t)E * 16);
  L.ovfg = o;  o = al256(o + (size_t)E * 4);
  L.total = o;
  return L;
}

__global__ __launch_bounds__(256) void k_prep(const u16* __restrict__ w1, const u16* __restrict__ w2,
                                              const u16* __restrict__ wr, int* __restrict__ cnt,
                                              u16* __restrict__ w1f, u16* __restrict__ wl,
                                              const u16* __restrict__ pos, u32x2* __restrict__ pos4,
                                              int N, int ngroups) {
  const int idx = (int)blockIdx.x * 256 + threadIdx.x;
  if (idx < 16384) {
    int j = idx & 7, lane = (idx >> 3) & 63, ks = (idx >> 9) & 3, t = idx >> 11;
    int q = lane >> 4, n = lane & 15;
    w1f[idx] = w1[(32 * ks + 8 * q + j) * 128 + 16 * t + n];
  } else if (idx < 16384 + 49152) {
    int i = idx - 16384;
    int j = i & 7, lane = (i >> 3) & 63, ks = (i >> 9) & 3, st = i >> 11;
    int w = st / 3, c = st % 3;
    int q = lane >> 4, n = lane & 15;
    int u = 16 * (2 * ks + (j >> 2)) + 4 * q + (j & 3);
    wl[i] = w2[u * 384 + 128 * c + 16 * w + n];
  } else if (idx < 16384 + 49152 + 6144) {
    int i = idx - 16384 - 49152;
    int j = i & 3, lane = (i >> 2) & 63, st = i >> 8;
    int w = st / 3, c = st % 3;
    int q = lane >> 4, n = lane & 15;
    wl[49152 + i] = wr[(4 * q + j) * 384 + 128 * c + 16 * w + n];
  }
  if (idx <= ngroups) cnt[idx * CNT_STRIDE] = 0;
  if (idx < N) {
    u32x2 p;
    p[0] = (u32)pos[idx * 3] | ((u32)pos[idx * 3 + 1] << 16);
    p[1] = (u32)pos[idx * 3 + 2];
    pos4[idx] = p;
  }
}

__global__ __launch_bounds__(256) void k_edges(
    const u16* __restrict__ sv, const u32x2* __restrict__ pos4, const int* __restrict__ src,
    const int* __restrict__ dst, const u16* __restrict__ w1f, int* __restrict__ cnt,
    u16* __restrict__ hbuf, const u16* __restrict__ freqs, int4* __restrict__ rec16,
    u32x4* __restrict__ rbfo, int4* __restrict__ ovf16, int* __restrict__ ovfg, int N, int E,
    int ngroups, int h_blocks) {
  __shared__ __attribute__((aligned(16))) u32x4 w1s[2048];
  if ((int)blockIdx.x < h_blocks) {
    const int lane = threadIdx.x & 63, wave = threadIdx.x >> 6;
    const int q = lane >> 4, n = lane & 15;
    const int node0 = ((int)blockIdx.x * 4 + wave) * 32;
    {
      const u32x4* g4 = reinterpret_cast<const u32x4*>(w1f);
      u32x4 tmp[8];
#pragma unroll
      for (int k = 0; k < 8; ++k) tmp[k] = g4[threadIdx.x + k * 256];
      bf16x8 sB[2][4];
#pragma unroll
      for (int u = 0; u < 2; ++u) {
        int node = node0 + 16 * u + n;
        if (node >= N) node = N - 1;
        const bf16x8* srow = reinterpret_cast<const bf16x8*>(sv + (size_t)node * 512);
#pragma unroll
        for (int ks = 0; ks < 4; ++ks) sB[u][ks] = srow[4 * ks + q];
      }
      __builtin_amdgcn_sched_barrier(0);
#pragma unroll
      for (int k = 0; k < 8; ++k) w1s[threadIdx.x + k * 256] = tmp[k];
      __syncthreads();
      const bf16x8* W1f = reinterpret_cast<const bf16x8*>(w1s);
      u32x4 hp[2][4];
#pragma unroll
      for (int t = 0; t < 8; ++t) {
        f32x4 d1[2] = {{0.f, 0.f, 0.f, 0.f}, {0.f, 0.f, 0.f, 0.f}};
#pragma unroll
        for (int ks = 0; ks < 4; ++ks) {
          const bf16x8 wfr = W1f[(t * 4 + ks) * 64 + lane];
          d1[0] = __builtin_amdgcn_mfma_f32_16x16x32_bf16(wfr, sB[0][ks], d1[0], 0, 0, 0);
          d1[1] = __builtin_amdgcn_mfma_f32_16x16x32_bf16(wfr, sB[1][ks], d1[1], 0, 0, 0);
        }
#pragma unroll
        for (int u = 0; u < 2; ++u) {
          float sl[4];
#pragma unroll
          for (int r = 0; r < 4; ++r) {
            const float a = rb(d1[u][r]);
            sl[r] = a * __builtin_amdgcn_rcpf(1.0f + __expf(-a));
          }
          hp[u][t >> 1][(t & 1) * 2 + 0] = pack2(sl[0], sl[1]);
          hp[u][t >> 1][(t & 1) * 2 + 1] = pack2(sl[2], sl[3]);
        }
      }
#pragma unroll
      for (int u = 0; u < 2; ++u) {
        const int node = node0 + 16 * u + n;
        if (node < N) {
          u32x4* hrow = reinterpret_cast<u32x4*>(hbuf + (size_t)node * 128);
#pragma unroll
          for (int ks = 0; ks < 4; ++ks) hrow[4 * ks + q] = hp[u][ks];
        }
      }
    }
    return;
  }
  u32x4* lst = w1s;
  int* lcnt = reinterpret_cast<int*>(w1s + 1024);
  if (threadIdx.x == 0) *lcnt = 0;
  __syncthreads();
  const int e0 = ((int)blockIdx.x - h_blocks) * 512 + threadIdx.x;
  int ee[2] = {e0, e0 + 256}, ss[2], dv[2];
#pragma unroll
  for (int u = 0; u < 2; ++u) {
    const bool ok = ee[u] < E;
    ss[u] = ok ? src[ee[u]] : 0;
    dv[u] = ok ? dst[ee[u]] : 0;
  }
  u32x2 ps[2], pd[2];
#pragma unroll
  for (int u = 0; u < 2; ++u) { ps[u] = pos4[ss[u]]; pd[u] = pos4[dv[u]]; }
#pragma unroll
  for (int u = 0; u < 2; ++u) {
    const float rx = rb(lo16(ps[u][0]) - lo16(pd[u][0]));
    const float ry = rb(hi16(ps[u][0]) - hi16(pd[u][0]));
    const float rz = rb(lo16(ps[u][1]) - lo16(pd[u][1]));
    float q2 = rb(rb(rx * rx) + rb(ry * ry) + rb(rz * rz));
    q2 = rb(q2 + 1e-8f);
    const float dd = rb(sqrtf(q2));
    if (ee[u] < E && dd < 5.0f) {
      const int p = atomicAdd(lcnt, 1);
      u32x4 a;
      a[0] = (u32)ee[u]; a[1] = (u32)ss[u]; a[2] = (u32)dv[u];
      a[3] = pack2(rx, ry);
      lst[2 * p] = a;
      lst[2 * p + 1][0] = pack2(rz, dd);
    }
  }
  __syncthreads();
  const int nact = *lcnt;
  for (int p = threadIdx.x; p < nact; p += 256) {
    const u32x4 a = lst[2 * p];
    const u32 zd = lst[2 * p + 1][0];
    const int e = (int)a[0], s = (int)a[1], d = (int)a[2];
    const float rx = lo16(a[3]), ry = hi16(a[3]), rz = lo16(zd), dd = hi16(zd);
    const int g = d >> 4;
    const int slot = atomicAdd(&cnt[g * CNT_STRIDE], 1);
    const float hx = rb(rx / dd), hy = rb(ry / dd), hz = rb(rz / dd);
    const float fc = rb(0.5f * rb(rb(cosf(rb(rb(3.141592653589793f * dd) / 5.0f))) + 1.0f));
    int4 r;
    r.x = (e << 4) | (d & 15);
    r.y = s;
    r.z = (int)pack2(hx, hy);
    r.w = (int)pack2(hz, fc);
    u32x4 rq[2];
#pragma unroll
    for (int k = 0; k < 8; ++k) {
      const u32 fq = *reinterpret_cast<const u32*>(freqs + 2 * k);
      const float s0 = rb(__sinf(rb(dd * lo16(fq))));
      const float s1 = rb(__sinf(rb(dd * hi16(fq))));
      rq[k >> 2][k & 3] = pack2(s0 / dd, s1 / dd);
    }
    size_t ridx;
    if (slot < CAP) {
      ridx = (size_t)g * CAP + slot;
      rec16[ridx] = r;
    } else {
      const int o = atomicAdd(&cnt[ngroups * CNT_STRIDE], 1);
      ovf16[o] = r;
      ovfg[o] = g;
      ridx = (size_t)ngroups * CAP + o;
    }
    rbfo[ridx * 2] = rq[0];
    rbfo[ridx * 2 + 1] = rq[1];
  }
}

__device__ __forceinline__ float bfx(const bf16x8& v, int i) {
  return __uint_as_float(((u32)(u16)v[i]) << 16);
}
__device__ __forceinline__ bf16x4 pack4(float a, float b, float c, float d) {
  u32x2 p;
  p[0] = pack2(a, b);
  p[1] = pack2(c, d);
  return *reinterpret_cast<bf16x4*>(&p);
}

#define RSLOTS 6
#define EDGE_STRIDE 1040
#define SLOT_RBF (16 * EDGE_STRIDE)
#define SLOT_BYTES (SLOT_RBF + 512)
#define L_REC (RSLOTS * SLOT_BYTES)
#define L_OBUF (L_REC + 8 * 5 * 128 * 4)
#define L_OWN (L_OBUF + 16384)
#define L_META (L_OWN + 16384)
#define K2_LDS (L_META + 64)

__device__ __forceinline__ void dma16(const void* src, unsigned lds_dst) {
  unsigned keep;
  const unsigned d = (unsigned)__builtin_amdgcn_readfirstlane((int)lds_dst);
  asm volatile("s_mov_b32 %0, m0\n\ts_mov_b32 m0, %2\n\ts_nop 0\n\tglobal_load_lds_dwordx4 %1, off\n\ts_mov_b32 m0, %0"
               : "=&s"(keep) : "v"(src), "s"(d) : "memory");
}
__device__ __forceinline__ void wait_vm(int n) {
  if (n >= 15) asm volatile("s_waitcnt vmcnt(15)" ::: "memory");
  else if (n >= 12) asm volatile("s_waitcnt vmcnt(12)" ::: "memory");
  else if (n >= 9) asm volatile("s_waitcnt vmcnt(9)" ::: "memory");
  else if (n >= 6) asm volatile("s_waitcnt vmcnt(6)" ::: "memory");
  else if (n >= 3) asm volatile("s_waitcnt vmcnt(3)" ::: "memory");
  else asm volatile("s_waitcnt vmcnt(0)" ::: "memory");
}
__device__ __forceinline__ void raw_barrier() {
  asm volatile("s_waitcnt lgkmcnt(0)" ::: "memory");
  __builtin_amdgcn_s_barrier();
  asm volatile("" ::: "memory");
}
__device__ __forceinline__ int uni(int x) { return __builtin_amdgcn_readfirstlane(x); }

__global__ __launch_bounds__(512) void k_message(
    const u16* __restrict__ sv, const int* __restrict__ cnt, const u16* __restrict__ wl_g,
    const u16* __restrict__ hbuf, const int4* __restrict__ rec16, const u16* __restrict__ rbfa,
    const int4* __restrict__ ovf16, const int* __restrict__ ovfg, u16* __restrict__ out, int N,
    int ngroups) {
  extern __shared__ __attribute__((aligned(16))) char smem[];
  const int tid = threadIdx.x;
  const int lane = tid & 63, wave = uni(tid >> 6);
  const int q = lane >> 4, n = lane & 15;
  const char* svb = reinterpret_cast<const char*>(sv);
  const char* hb = reinterpret_cast<const char*>(hbuf);
  const char* rbb = reinterpret_cast<const char*>(rbfa);
  const unsigned lds0 = (unsigned)(size_t)(__attribute__((address_space(3))) char*)smem;
  int* rec = reinterpret_cast<int*>(smem + L_REC);
  int* meta = reinterpret_cast<int*>(smem + L_META);

  bf16x8 w2[3][4];
  bf16x4 wr[3];
  {
    const bf16x8* W2g = reinterpret_cast<const bf16x8*>(wl_g);
    const bf16x4* Wrg = reinterpret_cast<const bf16x4*>(wl_g + 49152);
#pragma unroll
    for (int c = 0; c < 3; ++c) {
#pragma unroll
      for (int ks = 0; ks < 4; ++ks) w2[c][ks] = W2g[((wave * 3 + c) * 4 + ks) * 64 + lane];
      wr[c] = Wrg[(wave * 3 + c) * 64 + lane];
    }
  }

  auto run = [&](int nit, int gbase, int gstep, const char* ownsrc) {
    int pi = 0, pb = 0, issued = 0, t = 0;
    while (pi < nit && uni(meta[pi]) == 0) ++pi;
    auto issue = [&]() {
      const unsigned S = lds0 + (unsigned)((issued % RSLOTS) * SLOT_BYTES);
      const int* R = rec + pi * 640;
#pragma unroll
      for (int u = 0; u < 2; ++u) {
        const int e = 2 * wave + u;
        const u32 se = (u32)uni(R[128 + pb + e]);
        const char* src = (lane < 16) ? hb + (se * 256u + (u32)lane * 16u)
                                      : svb + (se * 1024u + 256u + (u32)(lane - 16) * 16u);
        dma16(src, S + (unsigned)(e * EDGE_STRIDE));
      }
      if (lane < 4) {
        const u32 ri = (u32)R[512 + pb + 2 * wave + (lane >> 1)];
        dma16(rbb + ((size_t)ri * 32u + (u32)(lane & 1) * 16u), S + (unsigned)(SLOT_RBF + wave * 64));
      }
      ++issued;
      pb += 16;
      while (pi < nit && pb >= uni(meta[pi])) { ++pi; pb = 0; }
    };
    for (int k = 0; k < RSLOTS - 1; ++k)
      if (pi < nit) issue();

    for (int i = 0; i < nit; ++i) {
      const int g = gbase + i * gstep;
      const int ci = uni(meta[i]);
      {
        const unsigned O = lds0 + L_OWN;
#pragma unroll
        for (int u = 0; u < 2; ++u) {
          int node = g * GROUP + 2 * wave + u;
          if (node >= N) node = N - 1;
          dma16(ownsrc + ((u32)node * 1024u + (u32)lane * 16u), O + (unsigned)((2 * wave + u) * 1024));
        }
      }
      const int issued0 = issued;
      f32x4 acc[4];
#pragma unroll
      for (int c = 0; c < 4; ++c) acc[c] = (f32x4){0.f, 0.f, 0.f, 0.f};
      const int* R = rec + i * 640;

      for (int base = 0; base < ci; base += 16) {
        wait_vm(3 * (issued - t - 1));
        raw_barrier();
        if (pi < nit) issue();
        const char* S = smem + (t % RSLOTS) * SLOT_BYTES;
        ++t;
        bf16x8 hA[4];
#pragma unroll
        for (int ks = 0; ks < 4; ++ks)
          hA[ks] = *reinterpret_cast<const bf16x8*>(S + n * EDGE_STRIDE + ks * 64 + q * 16);
        const bf16x4 rbfA = *reinterpret_cast<const bf16x4*>(S + SLOT_RBF + n * 32 + q * 8);
        const int4 key4 = *reinterpret_cast<const int4*>(R + base + 4 * q);
        const int4 hxy4 = *reinterpret_cast<const int4*>(R + 256 + base + 4 * q);
        const int4 hzf4 = *reinterpret_cast<const int4*>(R + 384 + base + 4 * q);
        float vv[3][4];
#pragma unroll
        for (int r = 0; r < 4; ++r)
#pragma unroll
          for (int x = 0; x < 3; ++x)
            vv[x][r] = bf2f(*reinterpret_cast<const u16*>(S + (4 * q + r) * EDGE_STRIDE + 256 + x * 256 +
                                                          (16 * wave + n) * 2));
        const int kk[4] = {key4.x, key4.y, key4.z, key4.w};
        const u32 xy[4] = {(u32)hxy4.x, (u32)hxy4.y, (u32)hxy4.z, (u32)hxy4.w};
        const u32 zf[4] = {(u32)hzf4.x, (u32)hzf4.y, (u32)hzf4.z, (u32)hzf4.w};
        bf16x4 selA;
#pragma unroll
        for (int j = 0; j < 4; ++j)
          selA[j] = (kk[j] >= 0 && (kk[j] & 15) == n) ? (short)(zf[j] >> 16) : (short)0;
        f32x4 m[3];
#pragma unroll
        for (int c = 0; c < 3; ++c) {
          f32x4 wf = {0.f, 0.f, 0.f, 0.f};
          wf = __builtin_amdgcn_mfma_f32_16x16x16bf16_1k(rbfA, wr[c], wf, 0, 0, 0);
          f32x4 d2 = {0.f, 0.f, 0.f, 0.f};
#pragma unroll
          for (int ks = 0; ks < 4; ++ks)
            d2 = __builtin_amdgcn_mfma_f32_16x16x32_bf16(hA[ks], w2[c][ks], d2, 0, 0, 0);
#pragma unroll
          for (int r = 0; r < 4; ++r) m[c][r] = d2[r] * wf[r];
        }
        acc[0] = __builtin_amdgcn_mfma_f32_16x16x16bf16_1k(
            selA, pack4(m[0][0], m[0][1], m[0][2], m[0][3]), acc[0], 0, 0, 0);
#pragma unroll
        for (int x = 0; x < 3; ++x) {
          float mv[4];
#pragma unroll
          for (int r = 0; r < 4; ++r) {
            const float hh = (x == 0) ? lo16(xy[r]) : ((x == 1) ? hi16(xy[r]) : lo16(zf[r]));
            mv[r] = m[1][r] * vv[x][r] + m[2][r] * hh;
          }
          acc[1 + x] = __builtin_amdgcn_mfma_f32_16x16x16bf16_1k(
              selA, pack4(mv[0], mv[1], mv[2], mv[3]), acc[1 + x], 0, 0, 0);
        }
      }
      {
        u16* ob = reinterpret_cast<u16*>(smem + L_OBUF);
#pragma unroll
        for (int c = 0; c < 4; ++c)
#pragma unroll
          for (int r = 0; r < 4; ++r) ob[(4 * q + r) * 512 + c * 128 + 16 * wave + n] = f2bf(acc[c][r]);
        wait_vm(3 * (issued - issued0));
        raw_barrier();
        const int node = g * GROUP + (tid >> 5);
        const u32x4* o4 = reinterpret_cast<const u32x4*>(smem + L_OBUF + tid * 32);
        const u32x4* w4 = reinterpret_cast<const u32x4*>(smem + L_OWN + tid * 32);
        u32x4 res[2];
#pragma unroll
        for (int h = 0; h < 2; ++h) {
          const u32x4 dl = o4[h], ow = w4[h];
#pragma unroll
          for (int k = 0; k < 4; ++k)
            res[h][k] = pack2(lo16(ow[k]) + lo16(dl[k]), hi16(ow[k]) + hi16(dl[k]));
        }
        if (node < N) {
          u32x4* dst = reinterpret_cast<u32x4*>(reinterpret_cast<char*>(out) + ((size_t)g * GROUP * 1024 + tid * 32));
          dst[0] = res[0];
          dst[1] = res[1];
        }
        raw_barrier();
      }
    }
    asm volatile("s_waitcnt vmcnt(0)" ::: "memory");
  };

  for (int b0 = (int)blockIdx.x; b0 < ngroups; b0 += (int)gridDim.x * 8) {
    const int gi = b0 + (int)gridDim.x * wave;
    int nit = (ngroups - b0 + (int)gridDim.x - 1) / (int)gridDim.x;
    if (nit > 8) nit = 8;
    if (gi < ngroups) {
      int* R = rec + wave * 640;
      const int total = cnt[gi * CNT_STRIDE];
      int4 r0 = rec16[(size_t)gi * CAP + lane], r1 = rec16[(size_t)gi * CAP + lane + 64];
      const int nb = total < CAP ? total : CAP;
      if (lane >= nb) r0.x = 0x7fffffff;
      if (lane + 64 >= nb) r1.x = 0x7fffffff;
      R[lane] = r0.x;
      R[lane + 64] = r1.x;
      __builtin_amdgcn_wave_barrier();
      int k0 = 0, k1 = 64;
      for (int i = 0; i < nb; i += 4) {
        const int4 kk = *reinterpret_cast<const int4*>(R + i);
        k0 += (kk.x < r0.x) + (kk.y < r0.x) + (kk.z < r0.x) + (kk.w < r0.x);
      }
      if (nb > 64) {
        k1 = 0;
        for (int i = 0; i < nb; i += 4) {
          const int4 kk = *reinterpret_cast<const int4*>(R + i);
          k1 += (kk.x < r1.x) + (kk.y < r1.x) + (kk.z < r1.x) + (kk.w < r1.x);
        }
      }
      if (lane >= nb) { k0 = lane; r0 = make_int4(-1, 0, 0, 0); }
      if (lane + 64 >= nb) { k1 = lane + 64; r1 = make_int4(-1, 0, 0, 0); }
      __builtin_amdgcn_wave_barrier();
      R[k0] = r0.x; R[128 + k0] = r0.y; R[256 + k0] = r0.z; R[384 + k0] = r0.w;
      R[512 + k0] = gi * CAP + (lane < nb ? lane : 0);
      R[k1] = r1.x; R[128 + k1] = r1.y; R[256 + k1] = r1.z; R[384 + k1] = r1.w;
      R[512 + k1] = gi * CAP + (lane + 64 < nb ? lane + 64 : 0);
      if (lane == 0) { meta[wave] = nb; meta[8 + wave] = total; }
    } else if (lane == 0) {
      meta[wave] = 0; meta[8 + wave] = 0;
    }
    __syncthreads();
    run(nit, b0, (int)gridDim.x, svb);

    int tot[8];
#pragma unroll
    for (int i = 0; i < 8; ++i) tot[i] = uni(meta[8 + i]);
    __syncthreads();
#pragma unroll 1
    for (int i = 0; i < nit; ++i) {
      int ti = 0;
#pragma unroll
      for (int j = 0; j < 8; ++j) ti = (i == j) ? tot[j] : ti;
      if (ti <= CAP) continue;
      const int g = b0 + i * (int)gridDim.x;
      const int ovf_n = cnt[ngroups * CNT_STRIDE];
      int pos_ = 0;
      while (pos_ < ovf_n) {
        if (wave == 0) {
          int have = 0;
          while (pos_ < ovf_n && have <= CAP - 64 - 16) {
            const int j = pos_ + lane;
            const bool mine = (j < ovf_n) && (ovfg[j] == g);
            const unsigned long long bal = __ballot(mine);
            const int before = __popcll(bal & ((1ull << lane) - 1ull));
            if (mine) {
              const int4 r = ovf16[j];
              const int p = have + before;
              rec[p] = r.x; rec[128 + p] = r.y; rec[256 + p] = r.z; rec[384 + p] = r.w;
              rec[512 + p] = ngroups * CAP + j;
            }
            have += __popcll(bal);
            pos_ += 64;
          }
          if (lane < 16) {
            const int p = have + lane;
            rec[p] = -1; rec[128 + p] = 0; rec[256 + p] = 0; rec[384 + p] = 0; rec[512 + p] = 0;
          }
          if (lane == 0) { meta[0] = have; meta[1] = pos_; }
        }
        __syncthreads();
        pos_ = uni(meta[1]);
        const int have = uni(meta[0]);
        __threadfence();
        if (have > 0) run(1, g, 0, reinterpret_cast<const char*>(out));
        __syncthreads();
      }
    }
    __syncthreads();
  }
}

extern "C" void kernel_launch(void* const* d_in, const int* in_sizes, int n_in,
                              void* d_out, int out_size, void* d_ws, size_t ws_size,
                              hipStream_t stream) {
  const u16* sv = (const u16*)d_in[0];
  const u16* pos = (const u16*)d_in[1];
  const int* src = (const int*)d_in[2];
  const int* dst = (const int*)d_in[3];
  const u16* freqs = (const u16*)d_in[4];
  const u16* w1 = (const u16*)d_in[5];
  const u16* w2 = (const u16*)d_in[6];
  const u16* wr = (const u16*)d_in[7];
  const int N = in_sizes[0] / 512;
  const int E = in_sizes[2];
  const int ngroups = (N + GROUP - 1) / GROUP;
  const WsLayout L = ws_layout(N, ngroups, E);
  char* ws = (char*)d_ws;
  int* cnt = (int*)(ws + L.cnt);
  u16* w1f = (u16*)(ws + L.w1f);
  u16* wl = (u16*)(ws + L.wl);
  u32x2* pos4 = (u32x2*)(ws + L.pos4);
  u16* hbuf = (u16*)(ws + L.h);
  int4* rec16 = (int4*)(ws + L.rec16);
  u16* rbfa = (u16*)(ws + L.rbf);
  int4* ovf16 = (int4*)(ws + L.ovf16);
  int* ovfg = (int*)(ws + L.ovfg);

  int prep_n = (71680 > ngroups + 1) ? 71680 : ngroups + 1;
  if (N > prep_n) prep_n = N;
  k_prep<<<(prep_n + 255) / 256, 256, 0, stream>>>(w1, w2, wr, cnt, w1f, wl, pos, pos4, N, ngroups);
  const int h_blocks = (N + 127) / 128;
  const int edge_blocks = (E + 511) / 512;
  k_edges<<<h_blocks + edge_blocks, 256, 0, stream>>>(sv, pos4, src, dst, w1f, cnt, hbuf, freqs, rec16,
                                                       (u32x4*)rbfa, ovf16, ovfg, N, E, ngroups, h_blocks);
  const size_t lds = K2_LDS;
  static_assert(K2_LDS <= 163840, "lds");
  int grid = 256;
  if (ngroups < grid) grid = ngroups;
  k_message<<<grid, 512, lds, stream>>>(sv, cnt, wl, hbuf, rec16, rbfa, ovf16, ovfg, (u16*)d_out, N,
                                        ngroups);
}
```

```cpp
#include <hip/hip_runtime.h>
#include <hip/hip_bf16.h>
#include <stdint.h>

typedef unsigned short u16;
typedef unsigned int u32;
using bf16x8 = __attribute__((ext_vector_type(8))) short;
using bf16x4 = __attribute__((ext_vector_type(4))) short;
using f32x4 = __attribute__((ext_vector_type(4))) float;
using f32x2 = __attribute__((ext_vector_type(2))) float;
using u32x4 = __attribute__((ext_vector_type(4))) unsigned int;
using u32x2 = __attribute__((ext_vector_type(2))) unsigned int;

#define GROUP 16
#define CAP 128
#define CNT_STRIDE 32
#define W1_ELEMS 16384
#define W2_BYTES 98304
#define WR_BYTES 12288
#define WL_BYTES (W2_BYTES + WR_BYTES)
#define K2_WAVES 8
#define SCR_BYTES 2560

__device__ __forceinline__ float bf2f(u16 x) { return __uint_as_float(((u32)x) << 16); }
__device__ __forceinline__ u16 f2bf(float x) {
  __hip_bfloat16 b = __float2bfloat16(x);
  return *reinterpret_cast<u16*>(&b);
}
__device__ __forceinline__ float rb(float x) { return bf2f(f2bf(x)); }
typedef __bf16 hbf2 __attribute__((ext_vector_type(2)));
__device__ __forceinline__ u32 pack2(float lo, float hi) {
  f32x2 f = {lo, hi};
  hbf2 b = __builtin_convertvector(f, hbf2);
  return *reinterpret_cast<u32*>(&b);
}
__device__ __forceinline__ float lo16(u32 x) { return __uint_as_float(x << 16); }
__device__ __forceinline__ float hi16(u32 x) { return __uint_as_float(x & 0xffff0000u); }

struct WsLayout {
  size_t cnt, w1f, wl, pos4, h, rec16, rbf, ovf16, ovfg, total;
};
static inline size_t al256(size_t x) { return (x + 255) & ~(size_t)255; }
static inline WsLayout ws_layout(int N, int ngroups, int E) {
  WsLayout L;
  size_t o = 0;
  L.cnt = o;   o = al256(o + (size_t)(ngroups + 1) * CNT_STRIDE * 4);
  L.w1f = o;   o = al256(o + W1_ELEMS * 2);
  L.wl = o;    o = al256(o + WL_BYTES);
  L.pos4 = o;  o = al256(o + (size_t)N * 8);
  L.h = o;     o = al256(o + (size_t)N * 256);
  L.rec16 = o; o = al256(o + (size_t)ngroups * CAP * 16);
  L.rbf = o;   o = al256(o + ((size_t)ngroups * CAP + (size_t)E) * 32);
  L.ovf16 = o; o = al256(o + (size_t)E * 16);
  L.ovfg = o;  o = al256(o + (size_t)E * 4);
  L.total = o;
  return L;
}

__global__ __launch_bounds__(256) void k_prep(const u16* __restrict__ w1, const u16* __restrict__ w2,
                                              const u16* __restrict__ wr, int* __restrict__ cnt,
                                              u16* __restrict__ w1f, u16* __restrict__ wl,
                                              const u16* __restrict__ pos, u32x2* __restrict__ pos4,
                                              int N, int ngroups) {
  const int idx = (int)blockIdx.x * 256 + threadIdx.x;
  if (idx < 16384) {
    int j = idx & 7, lane = (idx >> 3) & 63, ks = (idx >> 9) & 3, t = idx >> 11;
    int q = lane >> 4, n = lane & 15;
    w1f[idx] = w1[(32 * ks + 8 * q + j) * 128 + 16 * t + n];
  } else if (idx < 16384 + 49152) {
    int i = idx - 16384;
    int j = i & 7, lane = (i >> 3) & 63, ks = (i >> 9) & 3, st = i >> 11;
    int w = st / 3, c = st % 3;
    int q = lane >> 4, n = lane & 15;
    int u = 16 * (2 * ks + (j >> 2)) + 4 * q + (j & 3);
    wl[i] = w2[u * 384 + 128 * c + 16 * w + n];
  } else if (idx < 16384 + 49152 + 6144) {
    int i = idx - 16384 - 49152;
    int j = i & 3, lane = (i >> 2) & 63, st = i >> 8;
    int w = st / 3, c = st % 3;
    int q = lane >> 4, n = lane & 15;
    wl[49152 + i] = wr[(4 * q + j) * 384 + 128 * c + 16 * w + n];
  }
  if (idx <= ngroups) cnt[idx * CNT_STRIDE] = 0;
  if (idx < N) {
    u32x2 p;
    p[0] = (u32)pos[idx * 3] | ((u32)pos[idx * 3 + 1] << 16);
    p[1] = (u32)pos[idx * 3 + 2];
    pos4[idx] = p;
  }
}

__global__ __launch_bounds__(256) void k_edges(
    const u16* __restrict__ sv, const u32x2* __restrict__ pos4, const int* __restrict__ src,
    const int* __restrict__ dst, const u16* __restrict__ w1f, int* __restrict__ cnt,
    u16* __restrict__ hbuf, const u16* __restrict__ freqs, int4* __restrict__ rec16,
    u32x4* __restrict__ rbfo, int4* __restrict__ ovf16, int* __restrict__ ovfg, int N, int E,
    int ngroups, int h_blocks) {
  __shared__ __attribute__((aligned(16))) u32x4 w1s[2048];
  if ((int)blockIdx.x < h_blocks) {
    const int lane = threadIdx.x & 63, wave = threadIdx.x >> 6;
    const int q = lane >> 4, n = lane & 15;
    const int node0 = ((int)blockIdx.x * 4 + wave) * 32;
    {
      const u32x4* g4 = reinterpret_cast<const u32x4*>(w1f);
      u32x4 tmp[8];
#pragma unroll
      for (int k = 0; k < 8; ++k) tmp[k] = g4[threadIdx.x + k * 256];
      bf16x8 sB[2][4];
#pragma unroll
      for (int u = 0; u < 2; ++u) {
        int node = node0 + 16 * u + n;
        if (node >= N) node = N - 1;
        const bf16x8* srow = reinterpret_cast<const bf16x8*>(sv + (size_t)node * 512);
#pragma unroll
        for (int ks = 0; ks < 4; ++ks) sB[u][ks] = srow[4 * ks + q];
      }
      __builtin_amdgcn_sched_barrier(0);
#pragma unroll
      for (int k = 0; k < 8; ++k) w1s[threadIdx.x + k * 256] = tmp[k];
      __syncthreads();
      const bf16x8* W1f = reinterpret_cast<const bf16x8*>(w1s);
      u32x4 hp[2][4];
#pragma unroll
      for (int t = 0; t < 8; ++t) {
        f32x4 d1[2] = {{0.f, 0.f, 0.f, 0.f}, {0.f, 0.f, 0.f, 0.f}};
#pragma unroll
        for (int ks = 0; ks < 4; ++ks) {
          const bf16x8 wfr = W1f[(t * 4 + ks) * 64 + lane];
          d1[0] = __builtin_amdgcn_mfma_f32_16x16x32_bf16(wfr, sB[0][ks], d1[0], 0, 0, 0);
          d1[1] = __builtin_amdgcn_mfma_f32_16x16x32_bf16(wfr, sB[1][ks], d1[1], 0, 0, 0);
        }
#pragma unroll
        for (int u = 0; u < 2; ++u) {
          float sl[4];
#pragma unroll
          for (int r = 0; r < 4; ++r) {
            const float a = rb(d1[u][r]);
            sl[r] = a * __builtin_amdgcn_rcpf(1.0f + __expf(-a));
          }
          hp[u][t >> 1][(t & 1) * 2 + 0] = pack2(sl[0], sl[1]);
          hp[u][t >> 1][(t & 1) * 2 + 1] = pack2(sl[2], sl[3]);
        }
      }
#pragma unroll
      for (int u = 0; u < 2; ++u) {
        const int node = node0 + 16 * u + n;
        if (node < N) {
          u32x4* hrow = reinterpret_cast<u32x4*>(hbuf + (size_t)node * 128);
#pragma unroll
          for (int ks = 0; ks < 4; ++ks) hrow[4 * ks + q] = hp[u][ks];
        }
      }
    }
    return;
  }
  u32x4* lst = w1s;
  int* lcnt = reinterpret_cast<int*>(w1s + 1024);
  if (threadIdx.x == 0) *lcnt = 0;
  __syncthreads();
  const int e0 = ((int)blockIdx.x - h_blocks) * 512 + threadIdx.x;
  int ee[2] = {e0, e0 + 256}, ss[2], dv[2];
#pragma unroll
  for (int u = 0; u < 2; ++u) {
    const bool ok = ee[u] < E;
    ss[u] = ok ? src[ee[u]] : 0;
    dv[u] = ok ? dst[ee[u]] : 0;
  }
  u32x2 ps[2], pd[2];
#pragma unroll
  for (int u = 0; u < 2; ++u) { ps[u] = pos4[ss[u]]; pd[u] = pos4[dv[u]]; }
#pragma unroll
  for (int u = 0; u < 2; ++u) {
    const float rx = rb(lo16(ps[u][0]) - lo16(pd[u][0]));
    const float ry = rb(hi16(ps[u][0]) - hi16(pd[u][0]));
    const float rz = rb(lo16(ps[u][1]) - lo16(pd[u][1]));
    float q2 = rb(rb(rx * rx) + rb(ry * ry) + rb(rz * rz));
    q2 = rb(q2 + 1e-8f);
    const float dd = rb(sqrtf(q2));
    if (ee[u] < E && dd < 5.0f) {
      const int p = atomicAdd(lcnt, 1);
      u32x4 a;
      a[0] = (u32)ee[u]; a[1] = (u32)ss[u]; a[2] = (u32)dv[u];
      a[3] = pack2(rx, ry);
      lst[2 * p] = a;
      lst[2 * p + 1][0] = pack2(rz, dd);
    }
  }
  __syncthreads();
  const int nact = *lcnt;
  for (int p = threadIdx.x; p < nact; p += 256) {
    const u32x4 a = lst[2 * p];
    const u32 zd = lst[2 * p + 1][0];
    const int e = (int)a[0], s = (int)a[1], d = (int)a[2];
    const float rx = lo16(a[3]), ry = hi16(a[3]), rz = lo16(zd), dd = hi16(zd);
    const int g = d >> 4;
    const int slot = atomicAdd(&cnt[g * CNT_STRIDE], 1);
    const float hx = rb(rx / dd), hy = rb(ry / dd), hz = rb(rz / dd);
    const float fc = rb(0.5f * rb(rb(cosf(rb(rb(3.141592653589793f * dd) / 5.0f))) + 1.0f));
    int4 r;
    r.x = (e << 4) | (d & 15);
    r.y = s;
    r.z = (int)pack2(hx, hy);
    r.w = (int)pack2(hz, fc);
    u32x4 rq[2];
#pragma unroll
    for (int k = 0; k < 8; ++k) {
      const u32 fq = *reinterpret_cast<const u32*>(freqs + 2 * k);
      const float s0 = rb(__sinf(rb(dd * lo16(fq))));
      const float s1 = rb(__sinf(rb(dd * hi16(fq))));
      rq[k >> 2][k & 3] = pack2(s0 / dd, s1 / dd);
    }
    size_t ridx;
    if (slot < CAP) {
      ridx = (size_t)g * CAP + slot;
      rec16[ridx] = r;
    } else {
      const int o = atomicAdd(&cnt[ngroups * CNT_STRIDE], 1);
      ovf16[o] = r;
      ovfg[o] = g;
      ridx = (size_t)ngroups * CAP + o;
    }
    rbfo[ridx * 2] = rq[0];
    rbfo[ridx * 2 + 1] = rq[1];
  }
}

__device__ __forceinline__ float bfx(const bf16x8& v, int i) {
  return __uint_as_float(((u32)(u16)v[i]) << 16);
}
__device__ __forceinline__ bf16x4 pack4(float a, float b, float c, float d) {
  u32x2 p;
  p[0] = pack2(a, b);
  p[1] = pack2(c, d);
  return *reinterpret_cast<bf16x4*>(&p);
}

#define RSLOTS 6
#define EDGE_STRIDE 1040
#define SLOT_RBF (16 * EDGE_STRIDE)
#define SLOT_BYTES (SLOT_RBF + 512)
#define L_REC (RSLOTS * SLOT_BYTES)
#define L_OBUF (L_REC + 8 * 5 * 128 * 4)
#define L_OWN (L_OBUF + 16384)
#define L_META (L_OWN + 16384)
#define K2_LDS (L_META + 64)

__device__ __forceinline__ void dma16(const void* src, unsigned lds_dst) {
  unsigned keep;
  const unsigned d = (unsigned)__builtin_amdgcn_readfirstlane((int)lds_dst);
  asm volatile("s_mov_b32 %0, m0\n\ts_mov_b32 m0, %2\n\ts_nop 0\n\tglobal_load_lds_dwordx4 %1, off\n\ts_mov_b32 m0, %0"
               : "=&s"(keep) : "v"(src), "s"(d) : "memory");
}
__device__ __forceinline__ void wait_vm(int n) {
  if (n >= 15) asm volatile("s_waitcnt vmcnt(15)" ::: "memory");
  else if (n >= 12) asm volatile("s_waitcnt vmcnt(12)" ::: "memory");
  else if (n >= 9) asm volatile("s_waitcnt vmcnt(9)" ::: "memory");
  else if (n >= 6) asm volatile("s_waitcnt vmcnt(6)" ::: "memory");
  else if (n >= 3) asm volatile("s_waitcnt vmcnt(3)" ::: "memory");
  else asm volatile("s_waitcnt vmcnt(0)" ::: "memory");
}
__device__ __forceinline__ void raw_barrier() {
  asm volatile("s_waitcnt lgkmcnt(0)" ::: "memory");
  __builtin_amdgcn_s_barrier();
  asm volatile("" ::: "memory");
}
__device__ __forceinline__ int uni(int x) { return __builtin_amdgcn_readfirstlane(x); }

__global__ __launch_bounds__(512) void k_message(
    const u16* __restrict__ sv, const int* __restrict__ cnt, const u16* __restrict__ wl_g,
    const u16* __restrict__ hbuf, const int4* __restrict__ rec16, const u16* __restrict__ rbfa,
    const int4* __restrict__ ovf16, const int* __restrict__ ovfg, u16* __restrict__ out, int N,
    int ngroups) {
  extern __shared__ __attribute__((aligned(16))) char smem[];
  const int tid = threadIdx.x;
  const int lane = tid & 63, wave = uni(tid >> 6);
  const int q = lane >> 4, n = lane & 15;
  const char* svb = reinterpret_cast<const char*>(sv);
  const char* hb = reinterpret_cast<const char*>(hbuf);
  const char* rbb = reinterpret_cast<const char*>(rbfa);
  const unsigned lds0 = (unsigned)(size_t)(__attribute__((address_space(3))) char*)smem;
  int* rec = reinterpret_cast<int*>(smem + L_REC);
  int* meta = reinterpret_cast<int*>(smem + L_META);

  bf16x8 w2[3][4];
  bf16x4 wr[3];
  {
    const bf16x8* W2g = reinterpret_cast<const bf16x8*>(wl_g);
    const bf16x4* Wrg = reinterpret_cast<const bf16x4*>(wl_g + 49152);
#pragma unroll
    for (int c = 0; c < 3; ++c) {
#pragma unroll
      for (int ks = 0; ks < 4; ++ks) w2[c][ks] = W2g[((wave * 3 + c) * 4 + ks) * 64 + lane];
      wr[c] = Wrg[(wave * 3 + c) * 64 + lane];
    }
  }

  auto run = [&](int nit, int gbase, int gstep, const char* ownsrc) {
    int pi = 0, pb = 0, issued = 0, t = 0;
    while (pi < nit && uni(meta[pi]) == 0) ++pi;
    auto issue = [&]() {
      const unsigned S = lds0 + (unsigned)((issued % RSLOTS) * SLOT_BYTES);
      const int* R = rec + pi * 640;
#pragma unroll
      for (int u = 0; u < 2; ++u) {
        const int e = 2 * wave + u;
        const u32 se = (u32)uni(R[128 + pb + e]);
        const char* src = (lane < 16) ? hb + (se * 256u + (u32)lane * 16u)
                                      : svb + (se * 1024u + 256u + (u32)(lane - 16) * 16u);
        dma16(src, S + (unsigned)(e * EDGE_STRIDE));
      }
      if (lane < 4) {
        const u32 ri = (u32)R[512 + pb + 2 * wave + (lane >> 1)];
        dma16(rbb + ((size_t)ri * 32u + (u32)(lane & 1) * 16u), S + (unsigned)(SLOT_RBF + wave * 64));
      }
      ++issued;
      pb += 16;
      while (pi < nit && pb >= uni(meta[pi])) { ++pi; pb = 0; }
    };
    for (int k = 0; k < RSLOTS; ++k)
      if (pi < nit) issue();
    const int ntot = [&]() { int x = 0; for (int i = 0; i < nit; ++i) x += (uni(meta[i]) + 15) >> 4; return x; }();

    struct TR {
      bf16x8 hA[4];
      bf16x4 rbfA, selA;
      u32 xy[4], z01, z23;
      u32 vv[3][4];
    };
    int li = 0, lb = 0;
    while (li < nit && uni(meta[li]) == 0) ++li;
    auto load_tile = [&](int tt, TR& T) {
      const char* S = smem + (tt % RSLOTS) * SLOT_BYTES;
      const int* R = rec + li * 640;
#pragma unroll
      for (int ks = 0; ks < 4; ++ks)
        T.hA[ks] = *reinterpret_cast<const bf16x8*>(S + n * EDGE_STRIDE + ks * 64 + q * 16);
      T.rbfA = *reinterpret_cast<const bf16x4*>(S + SLOT_RBF + n * 32 + q * 8);
      const int4 key4 = *reinterpret_cast<const int4*>(R + lb + 4 * q);
      const int4 hxy4 = *reinterpret_cast<const int4*>(R + 256 + lb + 4 * q);
      const int4 hzf4 = *reinterpret_cast<const int4*>(R + 384 + lb + 4 * q);
#pragma unroll
      for (int r = 0; r < 4; ++r)
#pragma unroll
        for (int x = 0; x < 3; ++x)
          T.vv[x][r] = *reinterpret_cast<const u16*>(S + (4 * q + r) * EDGE_STRIDE + 256 + x * 256 +
                                                      (16 * wave + n) * 2);
      const int kk[4] = {key4.x, key4.y, key4.z, key4.w};
      const u32 zf[4] = {(u32)hzf4.x, (u32)hzf4.y, (u32)hzf4.z, (u32)hzf4.w};
#pragma unroll
      for (int j = 0; j < 4; ++j)
        T.selA[j] = (kk[j] >= 0 && (kk[j] & 15) == n) ? (short)(zf[j] >> 16) : (short)0;
      T.xy[0] = (u32)hxy4.x; T.xy[1] = (u32)hxy4.y; T.xy[2] = (u32)hxy4.z; T.xy[3] = (u32)hxy4.w;
      T.z01 = (zf[0] & 0xffffu) | (zf[1] << 16);
      T.z23 = (zf[2] & 0xffffu) | (zf[3] << 16);
      lb += 16;
      while (li < nit && lb >= uni(meta[li])) { ++li; lb = 0; }
    };

    f32x4 acc[4];
    int ci = 0, cb = 0, issued0 = 0;
    auto begin_item = [&]() {
      const int g = gbase + ci * gstep;
      const unsigned O = lds0 + L_OWN;
#pragma unroll
      for (int u = 0; u < 2; ++u) {
        int node = g * GROUP + 2 * wave + u;
        if (node >= N) node = N - 1;
        dma16(ownsrc + ((u32)node * 1024u + (u32)lane * 16u), O + (unsigned)((2 * wave + u) * 1024));
      }
      issued0 = issued;
#pragma unroll
      for (int c = 0; c < 4; ++c) acc[c] = (f32x4){0.f, 0.f, 0.f, 0.f};
    };
    auto flush_item = [&]() {
      const int g = gbase + ci * gstep;
      u16* ob = reinterpret_cast<u16*>(smem + L_OBUF);
#pragma unroll
      for (int c = 0; c < 4; ++c)
#pragma unroll
        for (int r = 0; r < 4; ++r) ob[(4 * q + r) * 512 + c * 128 + 16 * wave + n] = f2bf(acc[c][r]);
      wait_vm(3 * (issued - issued0));
      raw_barrier();
      const int node = g * GROUP + (tid >> 5);
      const u32x4* o4 = reinterpret_cast<const u32x4*>(smem + L_OBUF + tid * 32);
      const u32x4* w4 = reinterpret_cast<const u32x4*>(smem + L_OWN + tid * 32);
      u32x4 res[2];
#pragma unroll
      for (int h = 0; h < 2; ++h) {
        const u32x4 dl = o4[h], ow = w4[h];
#pragma unroll
        for (int k = 0; k < 4; ++k)
          res[h][k] = pack2(lo16(ow[k]) + lo16(dl[k]), hi16(ow[k]) + hi16(dl[k]));
      }
      if (node < N) {
        u32x4* dst = reinterpret_cast<u32x4*>(reinterpret_cast<char*>(out) + ((size_t)g * GROUP * 1024 + tid * 32));
        dst[0] = res[0];
        dst[1] = res[1];
      }
      raw_barrier();
    };
    auto skip_empty = [&]() {
      while (ci < nit && uni(meta[ci]) == 0) { begin_item(); flush_item(); ++ci; }
    };
    auto step = [&](TR& Tc, TR& Tn) {
      if (t + 1 < ntot) wait_vm(3 * (issued - t - 2));
      raw_barrier();
      if (pi < nit) issue();
      if (t + 1 < ntot) load_tile(t + 1, Tn);
      f32x4 m[3];
#pragma unroll
      for (int c = 0; c < 3; ++c) {
        f32x4 wf = {0.f, 0.f, 0.f, 0.f};
        wf = __builtin_amdgcn_mfma_f32_16x16x16bf16_1k(Tc.rbfA, wr[c], wf, 0, 0, 0);
        f32x4 d2 = {0.f, 0.f, 0.f, 0.f};
#pragma unroll
        for (int ks = 0; ks < 4; ++ks)
          d2 = __builtin_amdgcn_mfma_f32_16x16x32_bf16(Tc.hA[ks], w2[c][ks], d2, 0, 0, 0);
#pragma unroll
        for (int r = 0; r < 4; ++r) m[c][r] = d2[r] * wf[r];
      }
      acc[0] = __builtin_amdgcn_mfma_f32_16x16x16bf16_1k(
          Tc.selA, pack4(m[0][0], m[0][1], m[0][2], m[0][3]), acc[0], 0, 0, 0);
      const float hz[4] = {lo16(Tc.z01), hi16(Tc.z01), lo16(Tc.z23), hi16(Tc.z23)};
#pragma unroll
      for (int x = 0; x < 3; ++x) {
        float mv[4];
#pragma unroll
        for (int r = 0; r < 4; ++r) {
          const float hh = (x == 0) ? lo16(Tc.xy[r]) : ((x == 1) ? hi16(Tc.xy[r]) : hz[r]);
          mv[r] = m[1][r] * __uint_as_float(Tc.vv[x][r] << 16) + m[2][r] * hh;
        }
        acc[1 + x] = __builtin_amdgcn_mfma_f32_16x16x16bf16_1k(
            Tc.selA, pack4(mv[0], mv[1], mv[2], mv[3]), acc[1 + x], 0, 0, 0);
      }
      ++t;
      cb += 16;
      if (cb >= uni(meta[ci])) {
        flush_item();
        ++ci; cb = 0;
        skip_empty();
        if (ci < nit) begin_item();
      }
    };

    TR TA, TB;
    skip_empty();
    if (ntot > 0) {
      begin_item();
      wait_vm(3 * (issued - 1));
      raw_barrier();
      load_tile(0, TA);
      while (true) {
        step(TA, TB);
        if (t >= ntot) break;
        step(TB, TA);
        if (t >= ntot) break;
      }
    }
    asm volatile("s_waitcnt vmcnt(0)" ::: "memory");
  };

  for (int b0 = (int)blockIdx.x; b0 < ngroups; b0 += (int)gridDim.x * 8) {
    const int gi = b0 + (int)gridDim.x * wave;
    int nit = (ngroups - b0 + (int)gridDim.x - 1) / (int)gridDim.x;
    if (nit > 8) nit = 8;
    if (gi < ngroups) {
      int* R = rec + wave * 640;
      const int total = cnt[gi * CNT_STRIDE];
      int4 r0 = rec16[(size_t)gi * CAP + lane], r1 = rec16[(size_t)gi * CAP + lane + 64];
      const int nb = total < CAP ? total : CAP;
      if (lane >= nb) r0.x = 0x7fffffff;
      if (lane + 64 >= nb) r1.x = 0x7fffffff;
      R[lane] = r0.x;
      R[lane + 64] = r1.x;
      __builtin_amdgcn_wave_barrier();
      int k0 = 0, k1 = 64;
      for (int i = 0; i < nb; i += 4) {
        const int4 kk = *reinterpret_cast<const int4*>(R + i);
        k0 += (kk.x < r0.x) + (kk.y < r0.x) + (kk.z < r0.x) + (kk.w < r0.x);
      }
      if (nb > 64) {
        k1 = 0;
        for (int i = 0; i < nb; i += 4) {
          const int4 kk = *reinterpret_cast<const int4*>(R + i);
          k1 += (kk.x < r1.x) + (kk.y < r1.x) + (kk.z < r1.x) + (kk.w < r1.x);
        }
      }
      if (lane >= nb) { k0 = lane; r0 = make_int4(-1, 0, 0, 0); }
      if (lane + 64 >= nb) { k1 = lane + 64; r1 = make_int4(-1, 0, 0, 0); }
      __builtin_amdgcn_wave_barrier();
      R[k0] = r0.x; R[128 + k0] = r0.y; R[256 + k0] = r0.z; R[384 + k0] = r0.w;
      R[512 + k0] = gi * CAP + (lane < nb ? lane : 0);
      R[k1] = r1.x; R[128 + k1] = r1.y; R[256 + k1] = r1.z; R[384 + k1] = r1.w;
      R[512 + k1] = gi * CAP + (lane + 64 < nb ? lane + 64 : 0);
      if (lane == 0) { meta[wave] = nb; meta[8 + wave] = total; }
    } else if (lane == 0) {
      meta[wave] = 0; meta[8 + wave] = 0;
    }
    __syncthreads();
    run(nit, b0, (int)gridDim.x, svb);

    int tot[8];
#pragma unroll
    for (int i = 0; i < 8; ++i) tot[i] = uni(meta[8 + i]);
    __syncthreads();
#pragma unroll 1
    for (int i = 0; i < nit; ++i) {
      int ti = 0;
#pragma unroll
      for (int j = 0; j < 8; ++j) ti = (i == j) ? tot[j] : ti;
      if (ti <= CAP) continue;
      const int g = b0 + i * (int)gridDim.x;
      const int ovf_n = cnt[ngroups * CNT_STRIDE];
      int pos_ = 0;
      while (pos_ < ovf_n) {
        if (wave == 0) {
          int have = 0;
          while (pos_ < ovf_n && have <= CAP - 64 - 16) {
            const int j = pos_ + lane;
            const bool mine = (j < ovf_n) && (ovfg[j] == g);
            const unsigned long long bal = __ballot(mine);
            const int before = __popcll(bal & ((1ull << lane) - 1ull));
            if (mine) {
              const int4 r = ovf16[j];
              const int p = have + before;
              rec[p] = r.x; rec[128 + p] = r.y; rec[256 + p] = r.z; rec[384 + p] = r.w;
              rec[512 + p] = ngroups * CAP + j;
            }
            have += __popcll(bal);
            pos_ += 64;
          }
          if (lane < 16) {
            const int p = have + lane;
            rec[p] = -1; rec[128 + p] = 0; rec[256 + p] = 0; rec[384 + p] = 0; rec[512 + p] = 0;
          }
          if (lane == 0) { meta[0] = have; meta[1] = pos_; }
        }
        __syncthreads();
        pos_ = uni(meta[1]);
        const int have = uni(meta[0]);
        __threadfence();
        if (have > 0) run(1, g, 0, reinterpret_cast<const char*>(out));
        __syncthreads();
      }
    }
    __syncthreads();
  }
}

extern "C" void kernel_launch(void* const* d_in, const int* in_sizes, int n_in,
                              void* d_out, int out_size, void* d_ws, size_t ws_size,
                              hipStream_t stream) {
  const u16* sv = (const u16*)d_in[0];
  const u16* pos = (const u16*)d_in[1];
  const int* src = (const int*)d_in[2];
  const int* dst = (const int*)d_in[3];
  const u16* freqs = (const u16*)d_in[4];
  const u16* w1 = (const u16*)d_in[5];
  const u16* w2 = (const u16*)d_in[6];
  const u16* wr = (const u16*)d_in[7];
  const int N = in_sizes[0] / 512;
  const int E = in_sizes[2];
  const int ngroups = (N + GROUP - 1) / GROUP;
  const WsLayout L = ws_layout(N, ngroups, E);
  char* ws = (char*)d_ws;
  int* cnt = (int*)(ws + L.cnt);
  u16* w1f = (u16*)(ws + L.w1f);
  u16* wl = (u16*)(ws + L.wl);
  u32x2* pos4 = (u32x2*)(ws + L.pos4);
  u16* hbuf = (u16*)(ws + L.h);
  int4* rec16 = (int4*)(ws + L.rec16);
  u16* rbfa = (u16*)(ws + L.rbf);
  int4* ovf16 = (int4*)(ws + L.ovf16);
  int* ovfg = (int*)(ws + L.ovfg);

  int prep_n = (71680 > ngroups + 1) ? 71680 : ngroups + 1;
  if (N > prep_n) prep_n = N;
  k_prep<<<(prep_n + 255) / 256, 256, 0, stream>>>(w1, w2, wr, cnt, w1f, wl, pos, pos4, N, ngroups);
  const int h_blocks = (N + 127) / 128;
  const int edge_blocks = (E + 511) / 512;
  k_edges<<<h_blocks + edge_blocks, 256, 0, stream>>>(sv, pos4, src, dst, w1f, cnt, hbuf, freqs, rec16,
                                                       (u32x4*)rbfa, ovf16, ovfg, N, E, ngroups, h_blocks);
  const size_t lds = K2_LDS;
  static_assert(K2_LDS <= 163840, "lds");
  int grid = 256;
  if (ngroups < grid) grid = ngroups;
  k_message<<<grid, 512, lds, stream>>>(sv, cnt, wl, hbuf, rec16, rbfa, ovf16, ovfg, (u16*)d_out, N,
                                        ngroups);
}
```

```cpp
#include <hip/hip_runtime.h>
#include <hip/hip_bf16.h>
#include <stdint.h>

typedef unsigned short u16;
typedef unsigned int u32;
using bf16x8 = __attribute__((ext_vector_type(8))) short;
using bf16x4 = __attribute__((ext_vector_type(4))) short;
using f32x4 = __attribute__((ext_vector_type(4))) float;
using f32x2 = __attribute__((ext_vector_type(2))) float;
using u32x4 = __attribute__((ext_vector_type(4))) unsigned int;
using u32x2 = __attribute__((ext_vector_type(2))) unsigned int;

#define GROUP 16
#define CAP 128
#define CNT_STRIDE 32
#define W1_ELEMS 16384
#define W2_BYTES 98304
#define WR_BYTES 12288
#define WL_BYTES (W2_BYTES + WR_BYTES)
#define K2_WAVES 8
#define SCR_BYTES 2560

__device__ __forceinline__ float bf2f(u16 x) { return __uint_as_float(((u32)x) << 16); }
__device__ __forceinline__ u16 f2bf(float x) {
  __hip_bfloat16 b = __float2bfloat16(x);
  return *reinterpret_cast<u16*>(&b);
}
__device__ __forceinline__ float rb(float x) { return bf2f(f2bf(x)); }
typedef __bf16 hbf2 __attribute__((ext_vector_type(2)));
__device__ __forceinline__ u32 pack2(float lo, float hi) {
  f32x2 f = {lo, hi};
  hbf2 b = __builtin_convertvector(f, hbf2);
  return *reinterpret_cast<u32*>(&b);
}
__device__ __forceinline__ float lo16(u32 x) { return __uint_as_float(x << 16); }
__device__ __forceinline__ float hi16(u32 x) { return __uint_as_float(x & 0xffff0000u); }

struct WsLayout {
  size_t cnt, w1f, wl, pos4, h, rec16, rbf, ovf16, ovfg, total;
};
static inline size_t al256(size_t x) { return (x + 255) & ~(size_t)255; }
static inline WsLayout ws_layout(int N, int ngroups, int E) {
  WsLayout L;
  size_t o = 0;
  L.cnt = o;   o = al256(o + (size_t)(ngroups + 1) * CNT_STRIDE * 4);
  L.w1f = o;   o = al256(o + W1_ELEMS * 2);
  L.wl = o;    o = al256(o + WL_BYTES);
  L.pos4 = o;  o = al256(o + (size_t)N * 8);
  L.h = o;     o = al256(o + (size_t)N * 256);
  L.rec16 = o; o = al256(o + (size_t)ngroups * CAP * 16);
  L.rbf = o;   o = al256(o + ((size_t)ngroups * CAP + (size_t)E) * 32);
  L.ovf16 = o; o = al256(o + (size_t)E * 16);
  L.ovfg = o;  o = al256(o + (size_t)E * 4);
  L.total = o;
  return L;
}

__global__ __launch_bounds__(256) void k_prep(const u16* __restrict__ w1, const u16* __restrict__ w2,
                                              const u16* __restrict__ wr, int* __restrict__ cnt,
                                              u16* __restrict__ w1f, u16* __restrict__ wl,
                                              const u16* __restrict__ pos, u32x2* __restrict__ pos4,
                                              int N, int ngroups) {
  const int idx = (int)blockIdx.x * 256 + threadIdx.x;
  if (idx < 16384) {
    int j = idx & 7, lane = (idx >> 3) & 63, ks = (idx >> 9) & 3, t = idx >> 11;
    int q = lane >> 4, n = lane & 15;
    w1f[idx] = w1[(32 * ks + 8 * q + j) * 128 + 16 * t + n];
  } else if (idx < 16384 + 49152) {
    int i = idx - 16384;
    int j = i & 7, lane = (i >> 3) & 63, ks = (i >> 9) & 3, st = i >> 11;
    int w = st / 3, c = st % 3;
    int q = lane >> 4, n = lane & 15;
    int u = 16 * (2 * ks + (j >> 2)) + 4 * q + (j & 3);
    wl[i] = w2[u * 384 + 128 * c + 16 * w + n];
  } else if (idx < 16384 + 49152 + 6144) {
    int i = idx - 16384 - 49152;
    int j = i & 3, lane = (i >> 2) & 63, st = i >> 8;
    int w = st / 3, c = st % 3;
    int q = lane >> 4, n = lane & 15;
    wl[49152 + i] = wr[(4 * q + j) * 384 + 128 * c + 16 * w + n];
  }
  if (idx <= ngroups) cnt[idx * CNT_STRIDE] = 0;
  if (idx < N) {
    u32x2 p;
    p[0] = (u32)pos[idx * 3] | ((u32)pos[idx * 3 + 1] << 16);
    p[1] = (u32)pos[idx * 3 + 2];
    pos4[idx] = p;
  }
}

__global__ __launch_bounds__(256) void k_edges(
    const u16* __restrict__ sv, const u32x2* __restrict__ pos4, const int* __restrict__ src,
    const int* __restrict__ dst, const u16* __restrict__ w1f, int* __restrict__ cnt,
    u16* __restrict__ hbuf, const u16* __restrict__ freqs, int4* __restrict__ rec16,
    u32x4* __restrict__ rbfo, int4* __restrict__ ovf16, int* __restrict__ ovfg, int N, int E,
    int ngroups, int h_blocks) {
  __shared__ __attribute__((aligned(16))) u32x4 w1s[2048];
  if ((int)blockIdx.x < h_blocks) {
    const int lane = threadIdx.x & 63, wave = threadIdx.x >> 6;
    const int q = lane >> 4, n = lane & 15;
    const int node0 = ((int)blockIdx.x * 4 + wave) * 32;
    {
      const u32x4* g4 = reinterpret_cast<const u32x4*>(w1f);
      u32x4 tmp[8];
#pragma unroll
      for (int k = 0; k < 8; ++k) tmp[k] = g4[threadIdx.x + k * 256];
      bf16x8 sB[2][4];
#pragma unroll
      for (int u = 0; u < 2; ++u) {
        int node = node0 + 16 * u + n;
        if (node >= N) node = N - 1;
        const bf16x8* srow = reinterpret_cast<const bf16x8*>(sv + (size_t)node * 512);
#pragma unroll
        for (int ks = 0; ks < 4; ++ks) sB[u][ks] = srow[4 * ks + q];
      }
      __builtin_amdgcn_sched_barrier(0);
#pragma unroll
      for (int k = 0; k < 8; ++k) w1s[threadIdx.x + k * 256] = tmp[k];
      __syncthreads();
      const bf16x8* W1f = reinterpret_cast<const bf16x8*>(w1s);
      u32x4 hp[2][4];
#pragma unroll
      for (int t = 0; t < 8; ++t) {
        f32x4 d1[2] = {{0.f, 0.f, 0.f, 0.f}, {0.f, 0.f, 0.f, 0.f}};
#pragma unroll
        for (int ks = 0; ks < 4; ++ks) {
          const bf16x8 wfr = W1f[(t * 4 + ks) * 64 + lane];
          d1[0] = __builtin_amdgcn_mfma_f32_16x16x32_bf16(wfr, sB[0][ks], d1[0], 0, 0, 0);
          d1[1] = __builtin_amdgcn_mfma_f32_16x16x32_bf16(wfr, sB[1][ks], d1[1], 0, 0, 0);
        }
#pragma unroll
        for (int u = 0; u < 2; ++u) {
          float sl[4];
#pragma unroll
          for (int r = 0; r < 4; ++r) {
            const float a = rb(d1[u][r]);
            sl[r] = a * __builtin_amdgcn_rcpf(1.0f + __expf(-a));
          }
          hp[u][t >> 1][(t & 1) * 2 + 0] = pack2(sl[0], sl[1]);
          hp[u][t >> 1][(t & 1) * 2 + 1] = pack2(sl[2], sl[3]);
        }
      }
#pragma unroll
      for (int u = 0; u < 2; ++u) {
        const int node = node0 + 16 * u + n;
        if (node < N) {
          u32x4* hrow = reinterpret_cast<u32x4*>(hbuf + (size_t)node * 128);
#pragma unroll
          for (int ks = 0; ks < 4; ++ks) hrow[4 * ks + q] = hp[u][ks];
        }
      }
    }
    return;
  }
  u32x4* lst = w1s;
  int* lcnt = reinterpret_cast<int*>(w1s + 1024);
  if (threadIdx.x == 0) *lcnt = 0;
  __syncthreads();
  const int e0 = ((int)blockIdx.x - h_blocks) * 512 + threadIdx.x;
  int ee[2] = {e0, e0 + 256}, ss[2], dv[2];
#pragma unroll
  for (int u = 0; u < 2; ++u) {
    const bool ok = ee[u] < E;
    ss[u] = ok ? src[ee[u]] : 0;
    dv[u] = ok ? dst[ee[u]] : 0;
  }
  u32x2 ps[2], pd[2];
#pragma unroll
  for (int u = 0; u < 2; ++u) { ps[u] = pos4[ss[u]]; pd[u] = pos4[dv[u]]; }
#pragma unroll
  for (int u = 0; u < 2; ++u) {
    const float rx = rb(lo16(ps[u][0]) - lo16(pd[u][0]));
    const float ry = rb(hi16(ps[u][0]) - hi16(pd[u][0]));
    const float rz = rb(lo16(ps[u][1]) - lo16(pd[u][1]));
    float q2 = rb(rb(rx * rx) + rb(ry * ry) + rb(rz * rz));
    q2 = rb(q2 + 1e-8f);
    const float dd = rb(sqrtf(q2));
    if (ee[u] < E && dd < 5.0f) {
      const int p = atomicAdd(lcnt, 1);
      u32x4 a;
      a[0] = (u32)ee[u]; a[1] = (u32)ss[u]; a[2] = (u32)dv[u];
      a[3] = pack2(rx, ry);
      lst[2 * p] = a;
      lst[2 * p + 1][0] = pack2(rz, dd);
    }
  }
  __syncthreads();
  const int nact = *lcnt;
  for (int p = threadIdx.x; p < nact; p += 256) {
    const u32x4 a = lst[2 * p];
    const u32 zd = lst[2 * p + 1][0];
    const int e = (int)a[0], s = (int)a[1], d = (int)a[2];
    const float rx = lo16(a[3]), ry = hi16(a[3]), rz = lo16(zd), dd = hi16(zd);
    const int g = d >> 4;
    const int slot = atomicAdd(&cnt[g * CNT_STRIDE], 1);
    const float hx = rb(rx / dd), hy = rb(ry / dd), hz = rb(rz / dd);
    const float fc = rb(0.5f * rb(rb(cosf(rb(rb(3.141592653589793f * dd) / 5.0f))) + 1.0f));
    int4 r;
    r.x = (e << 4) | (d & 15);
    r.y = s;
    r.z = (int)pack2(hx, hy);
    r.w = (int)pack2(hz, fc);
    u32x4 rq[2];
#pragma unroll
    for (int k = 0; k < 8; ++k) {
      const u32 fq = *reinterpret_cast<const u32*>(freqs + 2 * k);
      const float s0 = rb(__sinf(rb(dd * lo16(fq))));
      const float s1 = rb(__sinf(rb(dd * hi16(fq))));
      rq[k >> 2][k & 3] = pack2(s0 / dd, s1 / dd);
    }
    size_t ridx;
    if (slot < CAP) {
      ridx = (size_t)g * CAP + slot;
      rec16[ridx] = r;
    } else {
      const int o = atomicAdd(&cnt[ngroups * CNT_STRIDE], 1);
      ovf16[o] = r;
      ovfg[o] = g;
      ridx = (size_t)ngroups * CAP + o;
    }
    rbfo[ridx * 2] = rq[0];
    rbfo[ridx * 2 + 1] = rq[1];
  }
}

__device__ __forceinline__ float bfx(const bf16x8& v, int i) {
  return __uint_as_float(((u32)(u16)v[i]) << 16);
}
__device__ __forceinline__ bf16x4 pack4(float a, float b, float c, float d) {
  u32x2 p;
  p[0] = pack2(a, b);
  p[1] = pack2(c, d);
  return *reinterpret_cast<bf16x4*>(&p);
}

#define RSLOTS 6
#define EDGE_STRIDE 1040
#define SLOT_RBF (16 * EDGE_STRIDE)
#define SLOT_BYTES (SLOT_RBF + 512)
#define L_REC (RSLOTS * SLOT_BYTES)
#define L_OBUF (L_REC + 8 * 5 * 128 * 4)
#define L_OWN (L_OBUF + 16384)
#define L_META (L_OWN + 16384)
#define K2_LDS (L_META + 64)

__device__ __forceinline__ void dma16(const void* src, unsigned lds_dst) {
  unsigned keep;
  const unsigned d = (unsigned)__builtin_amdgcn_readfirstlane((int)lds_dst);
  asm volatile("s_mov_b32 %0, m0\n\ts_mov_b32 m0, %2\n\ts_nop 0\n\tglobal_load_lds_dwordx4 %1, off\n\ts_mov_b32 m0, %0"
               : "=&s"(keep) : "v"(src), "s"(d) : "memory");
}
__device__ __forceinline__ void wait_vm(int n) {
  if (n >= 15) asm volatile("s_waitcnt vmcnt(15)" ::: "memory");
  else if (n >= 12) asm volatile("s_waitcnt vmcnt(12)" ::: "memory");
  else if (n >= 9) asm volatile("s_waitcnt vmcnt(9)" ::: "memory");
  else if (n >= 6) asm volatile("s_waitcnt vmcnt(6)" ::: "memory");
  else if (n >= 3) asm volatile("s_waitcnt vmcnt(3)" ::: "memory");
  else asm volatile("s_waitcnt vmcnt(0)" ::: "memory");
}
__device__ __forceinline__ void raw_barrier() {
  asm volatile("s_waitcnt lgkmcnt(0)" ::: "memory");
  __builtin_amdgcn_s_barrier();
  asm volatile("" ::: "memory");
}
__device__ __forceinline__ int uni(int x) { return __builtin_amdgcn_readfirstlane(x); }

__global__ __launch_bounds__(512) void k_message(
    const u16* __restrict__ sv, const int* __restrict__ cnt, const u16* __restrict__ wl_g,
    const u16* __restrict__ hbuf, const int4* __restrict__ rec16, const u16* __restrict__ rbfa,
    const int4* __restrict__ ovf16, const int* __restrict__ ovfg, u16* __restrict__ out, int N,
    int ngroups) {
  extern __shared__ __attribute__((aligned(16))) char smem[];
  const int tid = threadIdx.x;
  const int lane = tid & 63, wave = uni(tid >> 6);
  const int q = lane >> 4, n = lane & 15;
  const char* svb = reinterpret_cast<const char*>(sv);
  const char* hb = reinterpret_cast<const char*>(hbuf);
  const char* rbb = reinterpret_cast<const char*>(rbfa);
  const unsigned lds0 = (unsigned)(size_t)(__attribute__((address_space(3))) char*)smem;
  int* rec = reinterpret_cast<int*>(smem + L_REC);
  int* meta = reinterpret_cast<int*>(smem + L_META);

  bf16x8 w2[3][4];
  bf16x4 wr[3];
  {
    const bf16x8* W2g = reinterpret_cast<const bf16x8*>(wl_g);
    const bf16x4* Wrg = reinterpret_cast<const bf16x4*>(wl_g + 49152);
#pragma unroll
    for (int c = 0; c < 3; ++c) {
#pragma unroll
      for (int ks = 0; ks < 4; ++ks) w2[c][ks] = W2g[((wave * 3 + c) * 4 + ks) * 64 + lane];
      wr[c] = Wrg[(wave * 3 + c) * 64 + lane];
    }
  }

  auto run = [&](int nit, int gbase, int gstep, const char* ownsrc) {
    const u32 clo = (u32)uni(meta[0] | (meta[1] << 8) | (meta[2] << 16) | (meta[3] << 24));
    const u32 chi = (u32)uni(meta[4] | (meta[5] << 8) | (meta[6] << 16) | (meta[7] << 24));
    auto cntOf = [&](int i) {
      const u32 w = (i & 4) ? chi : clo;
      return (i < nit) ? (int)((w >> (8 * (i & 3))) & 255u) : 0;
    };
    int ntot = 0;
    for (int i = 0; i < nit; ++i) ntot += (cntOf(i) + 15) >> 4;

    int pi = 0, pb = 0, issued = 0, t = 0;
    while (pi < nit && cntOf(pi) == 0) ++pi;
    u32 d_s0 = 0, d_s1 = 0, d_ri = 0;
    auto load_ids = [&]() {
      if (pi < nit) {
        const int* R = rec + pi * 640;
        d_s0 = (u32)R[128 + pb + 2 * wave];
        d_s1 = (u32)R[128 + pb + 2 * wave + 1];
        d_ri = (u32)R[512 + pb + 2 * wave + ((lane >> 1) & 1)];
      }
    };
    auto issue = [&]() {
      const unsigned S = lds0 + (unsigned)((issued % RSLOTS) * SLOT_BYTES);
      int lane = tid & 63;
      asm volatile("" : "+v"(lane));
      const u32 lo = (lane < 16) ? (u32)lane * 16u : 256u + (u32)(lane - 16) * 16u;
      const char* b0p = (lane < 16) ? hb + (d_s0 * 256u + lo) : svb + (d_s0 * 1024u + lo);
      const char* b1p = (lane < 16) ? hb + (d_s1 * 256u + lo) : svb + (d_s1 * 1024u + lo);
      dma16(b0p, S + (unsigned)((2 * wave) * EDGE_STRIDE));
      dma16(b1p, S + (unsigned)((2 * wave + 1) * EDGE_STRIDE));
      if (lane < 4)
        dma16(rbb + ((size_t)d_ri * 32u + (u32)(lane & 1) * 16u), S + (unsigned)(SLOT_RBF + wave * 64));
      ++issued;
      pb += 16;
      while (pi < nit && pb >= cntOf(pi)) { ++pi; pb = 0; }
    };
    for (int k = 0; k < RSLOTS; ++k)
      if (pi < nit) { load_ids(); issue(); }

    struct TR {
      bf16x8 hA[4];
      bf16x4 rbfA;
      int4 key4, hxy4, hzf4;
      u32 vv[3][4];
    };
    int li = 0, lb = 0;
    while (li < nit && cntOf(li) == 0) ++li;
    auto load_tile = [&](int tt, TR& T) {
      const char* S = smem + (tt % RSLOTS) * SLOT_BYTES;
      const int* R = rec + li * 640;
      int ll = tid & 63;
      asm volatile("" : "+v"(ll));
      const int q = ll >> 4, n = ll & 15;
#pragma unroll
      for (int ks = 0; ks < 4; ++ks)
        T.hA[ks] = *reinterpret_cast<const bf16x8*>(S + n * EDGE_STRIDE + ks * 64 + q * 16);
      T.rbfA = *reinterpret_cast<const bf16x4*>(S + SLOT_RBF + n * 32 + q * 8);
      T.key4 = *reinterpret_cast<const int4*>(R + lb + 4 * q);
      T.hxy4 = *reinterpret_cast<const int4*>(R + 256 + lb + 4 * q);
      T.hzf4 = *reinterpret_cast<const int4*>(R + 384 + lb + 4 * q);
#pragma unroll
      for (int r = 0; r < 4; ++r)
#pragma unroll
        for (int x = 0; x < 3; ++x)
          T.vv[x][r] = *reinterpret_cast<const u16*>(S + (4 * q + r) * EDGE_STRIDE + 256 + x * 256 +
                                                      (16 * wave + n) * 2);
      lb += 16;
      while (li < nit && lb >= cntOf(li)) { ++li; lb = 0; }
    };

    f32x4 acc[4];
    int ci = 0, cb = 0, issued0 = 0;
    auto begin_item = [&]() {
      const int g = gbase + ci * gstep;
      const unsigned O = lds0 + L_OWN;
#pragma unroll
      for (int u = 0; u < 2; ++u) {
        int node = g * GROUP + 2 * wave + u;
        if (node >= N) node = N - 1;
        dma16(ownsrc + ((u32)node * 1024u + (u32)lane * 16u), O + (unsigned)((2 * wave + u) * 1024));
      }
      issued0 = issued;
#pragma unroll
      for (int c = 0; c < 4; ++c) acc[c] = (f32x4){0.f, 0.f, 0.f, 0.f};
    };
    auto flush_item = [&]() {
      const int g = gbase + ci * gstep;
      u16* ob = reinterpret_cast<u16*>(smem + L_OBUF);
      int tid = threadIdx.x;
      asm volatile("" : "+v"(tid));
      const int q = (tid & 63) >> 4, n = tid & 15;
#pragma unroll
      for (int c = 0; c < 4; ++c)
#pragma unroll
        for (int r = 0; r < 4; ++r) ob[(4 * q + r) * 512 + c * 128 + 16 * wave + n] = f2bf(acc[c][r]);
      wait_vm(3 * (issued - issued0));
      raw_barrier();
      const int node = g * GROUP + (tid >> 5);
      const u32x4* o4 = reinterpret_cast<const u32x4*>(smem + L_OBUF + tid * 32);
      const u32x4* w4 = reinterpret_cast<const u32x4*>(smem + L_OWN + tid * 32);
      u32x4 res[2];
#pragma unroll
      for (int h = 0; h < 2; ++h) {
        const u32x4 dl = o4[h], ow = w4[h];
#pragma unroll
        for (int k = 0; k < 4; ++k)
          res[h][k] = pack2(lo16(ow[k]) + lo16(dl[k]), hi16(ow[k]) + hi16(dl[k]));
      }
      if (node < N) {
        u32x4* dst = reinterpret_cast<u32x4*>(reinterpret_cast<char*>(out) + ((size_t)g * GROUP * 1024 + tid * 32));
        dst[0] = res[0];
        dst[1] = res[1];
      }
      raw_barrier();
    };
    auto skip_empty = [&]() {
      while (ci < nit && cntOf(ci) == 0) { begin_item(); flush_item(); ++ci; }
    };
    auto step = [&](TR& Tc, TR& Tn) {
      if (t + 1 < ntot) wait_vm(3 * (issued - t - 2));
      raw_barrier();
      if (pi < nit) issue();
      if (t + 1 < ntot) load_tile(t + 1, Tn);
      load_ids();
      __builtin_amdgcn_sched_barrier(0);
      f32x4 m[3];
#pragma unroll
      for (int c = 0; c < 3; ++c) {
        f32x4 wf = {0.f, 0.f, 0.f, 0.f};
        wf = __builtin_amdgcn_mfma_f32_16x16x16bf16_1k(Tc.rbfA, wr[c], wf, 0, 0, 0);
        f32x4 d2 = {0.f, 0.f, 0.f, 0.f};
#pragma unroll
        for (int ks = 0; ks < 4; ++ks)
          d2 = __builtin_amdgcn_mfma_f32_16x16x32_bf16(Tc.hA[ks], w2[c][ks], d2, 0, 0, 0);
#pragma unroll
        for (int r = 0; r < 4; ++r) m[c][r] = d2[r] * wf[r];
      }
      const int kk[4] = {Tc.key4.x, Tc.key4.y, Tc.key4.z, Tc.key4.w};
      const u32 xy[4] = {(u32)Tc.hxy4.x, (u32)Tc.hxy4.y, (u32)Tc.hxy4.z, (u32)Tc.hxy4.w};
      const u32 zf[4] = {(u32)Tc.hzf4.x, (u32)Tc.hzf4.y, (u32)Tc.hzf4.z, (u32)Tc.hzf4.w};
      bf16x4 selA;
#pragma unroll
      for (int j = 0; j < 4; ++j)
        selA[j] = (kk[j] >= 0 && (kk[j] & 15) == n) ? (short)(zf[j] >> 16) : (short)0;
      acc[0] = __builtin_amdgcn_mfma_f32_16x16x16bf16_1k(
          selA, pack4(m[0][0], m[0][1], m[0][2], m[0][3]), acc[0], 0, 0, 0);
#pragma unroll
      for (int x = 0; x < 3; ++x) {
        float mv[4];
#pragma unroll
        for (int r = 0; r < 4; ++r) {
          const float hh = (x == 0) ? lo16(xy[r]) : ((x == 1) ? hi16(xy[r]) : lo16(zf[r]));
          mv[r] = m[1][r] * __uint_as_float(Tc.vv[x][r] << 16) + m[2][r] * hh;
        }
        acc[1 + x] = __builtin_amdgcn_mfma_f32_16x16x16bf16_1k(
            selA, pack4(mv[0], mv[1], mv[2], mv[3]), acc[1 + x], 0, 0, 0);
      }
      __builtin_amdgcn_sched_barrier(0);
      ++t;
      cb += 16;
      if (cb >= cntOf(ci)) {
        flush_item();
        ++ci; cb = 0;
        skip_empty();
        if (ci < nit) begin_item();
      }
    };

    TR TA, TB;
    skip_empty();
    if (ntot > 0) {
      begin_item();
      wait_vm(3 * (issued - 1));
      raw_barrier();
      load_tile(0, TA);
      load_ids();
      while (true) {
        step(TA, TB);
        if (t >= ntot) break;
        step(TB, TA);
        if (t >= ntot) break;
      }
    }
    asm volatile("s_waitcnt vmcnt(0)" ::: "memory");
  };

  for (int b0 = (int)blockIdx.x; b0 < ngroups; b0 += (int)gridDim.x * 8) {
    const int gi = b0 + (int)gridDim.x * wave;
    int nit = (ngroups - b0 + (int)gridDim.x - 1) / (int)gridDim.x;
    if (nit > 8) nit = 8;
    if (gi < ngroups) {
      int* R = rec + wave * 640;
      const int total = cnt[gi * CNT_STRIDE];
      int4 r0 = rec16[(size_t)gi * CAP + lane], r1 = rec16[(size_t)gi * CAP + lane + 64];
      const int nb = total < CAP ? total : CAP;
      if (lane >= nb) r0.x = 0x7fffffff;
      if (lane + 64 >= nb) r1.x = 0x7fffffff;
      R[lane] = r0.x;
      R[lane + 64] = r1.x;
      __builtin_amdgcn_wave_barrier();
      int k0 = 0, k1 = 64;
      for (int i = 0; i < nb; i += 4) {
        const int4 kk = *reinterpret_cast<const int4*>(R + i);
        k0 += (kk.x < r0.x) + (kk.y < r0.x) + (kk.z < r0.x) + (kk.w < r0.x);
      }
      if (nb > 64) {
        k1 = 0;
        for (int i = 0; i < nb; i += 4) {
          const int4 kk = *reinterpret_cast<const int4*>(R + i);
          k1 += (kk.x < r1.x) + (kk.y < r1.x) + (kk.z < r1.x) + (kk.w < r1.x);
        }
      }
      if (lane >= nb) { k0 = lane; r0 = make_int4(-1, 0, 0, 0); }
      if (lane + 64 >= nb) { k1 = lane + 64; r1 = make_int4(-1, 0, 0, 0); }
      __builtin_amdgcn_wave_barrier();
      R[k0] = r0.x; R[128 + k0] = r0.y; R[256 + k0] = r0.z; R[384 + k0] = r0.w;
      R[512 + k0] = gi * CAP + (lane < nb ? lane : 0);
      R[k1] = r1.x; R[128 + k1] = r1.y; R[256 + k1] = r1.z; R[384 + k1] = r1.w;
      R[512 + k1] = gi * CAP + (lane + 64 < nb ? lane + 64 : 0);
      if (lane == 0) { meta[wave] = nb; meta[8 + wave] = total; }
    } else if (lane == 0) {
      meta[wave] = 0; meta[8 + wave] = 0;
    }
    __syncthreads();
    int ovm = 0;
#pragma unroll
    for (int i = 0; i < 8; ++i) ovm |= (uni(meta[8 + i]) > CAP) ? (1 << i) : 0;

    int oi = -1, pos_ = 0;
#pragma unroll 1
    while (true) {
      int r_nit = nit, r_g = b0, r_step = (int)gridDim.x;
      const char* r_own = svb;
      if (oi >= 0) {
        const int ovf_n = cnt[ngroups * CNT_STRIDE];
        while (oi < nit) {
          if (((ovm >> oi) & 1) && pos_ < ovf_n) break;
          ++oi; pos_ = 0;
        }
        if (oi >= nit) break;
        const int g = b0 + oi * (int)gridDim.x;
        __syncthreads();
        if (wave == 0) {
          int have = 0;
          while (pos_ < ovf_n && have <= CAP - 64 - 16) {
            const int j = pos_ + lane;
            const bool mine = (j < ovf_n) && (ovfg[j] == g);
            const unsigned long long bal = __ballot(mine);
            const int before = __popcll(bal & ((1ull << lane) - 1ull));
            if (mine) {
              const int4 r = ovf16[j];
              const int p = have + before;
              rec[p] = r.x; rec[128 + p] = r.y; rec[256 + p] = r.z; rec[384 + p] = r.w;
              rec[512 + p] = ngroups * CAP + j;
            }
            have += __popcll(bal);
            pos_ += 64;
          }
          if (lane < 16) {
            const int p = have + lane;
            rec[p] = -1; rec[128 + p] = 0; rec[256 + p] = 0; rec[384 + p] = 0; rec[512 + p] = 0;
          }
          if (lane == 0) { meta[0] = have; meta[1] = pos_; }
        }
        __syncthreads();
        pos_ = uni(meta[1]);
        __threadfence();
        r_nit = 1; r_g = g; r_step = 0; r_own = reinterpret_cast<const char*>(out);
      }
      run(r_nit, r_g, r_step, r_own);
      if (oi < 0) oi = 0;
    }
    __syncthreads();
  }
}

extern "C" void kernel_launch(void* const* d_in, const int* in_sizes, int n_in,
                              void* d_out, int out_size, void* d_ws, size_t ws_size,
                              hipStream_t stream) {
  const u16* sv = (const u16*)d_in[0];
  const u16* pos = (const u16*)d_in[1];
  const int* src = (const int*)d_in[2];
  const int* dst = (const int*)d_in[3];
  const u16* freqs = (const u16*)d_in[4];
  const u16* w1 = (const u16*)d_in[5];
  const u16* w2 = (const u16*)d_in[6];
  const u16* wr = (const u16*)d_in[7];
  const int N = in_sizes[0] / 512;
  const int E = in_sizes[2];
  const int ngroups = (N + GROUP - 1) / GROUP;
  const WsLayout L = ws_layout(N, ngroups, E);
  char* ws = (char*)d_ws;
  int* cnt = (int*)(ws + L.cnt);
  u16* w1f = (u16*)(ws + L.w1f);
  u16* wl = (u16*)(ws + L.wl);
  u32x2* pos4 = (u32x2*)(ws + L.pos4);
  u16* hbuf = (u16*)(ws + L.h);
  int4* rec16 = (int4*)(ws + L.rec16);
  u16* rbfa = (u16*)(ws + L.rbf);
  int4* ovf16 = (int4*)(ws + L.ovf16);
  int* ovfg = (int*)(ws + L.ovfg);

  int prep_n = (71680 > ngroups + 1) ? 71680 : ngroups + 1;
  if (N > prep_n) prep_n = N;
  k_prep<<<(prep_n + 255) / 256, 256, 0, stream>>>(w1, w2, wr, cnt, w1f, wl, pos, pos4, N, ngroups);
  const int h_blocks = (N + 127) / 128;
  const int edge_blocks = (E + 511) / 512;
  k_edges<<<h_blocks + edge_blocks, 256, 0, stream>>>(sv, pos4, src, dst, w1f, cnt, hbuf, freqs, rec16,
                                                       (u32x4*)rbfa, ovf16, ovfg, N, E, ngroups, h_blocks);
  const size_t lds = K2_LDS;
  static_assert(K2_LDS <= 163840, "lds");
  int grid = 256;
  if (ngroups < grid) grid = ngroups;
  k_message<<<grid, 512, lds, stream>>>(sv, cnt, wl, hbuf, rec16, rbfa, ovf16, ovfg, (u16*)d_out, N,
                                        ngroups);
}
```

```cpp
#include <hip/hip_runtime.h>
#include <hip/hip_bf16.h>
#include <stdint.h>

typedef unsigned short u16;
typedef unsigned int u32;
using bf16x8 = __attribute__((ext_vector_type(8))) short;
using bf16x4 = __attribute__((ext_vector_type(4))) short;
using f32x4 = __attribute__((ext_vector_type(4))) float;
using f32x2 = __attribute__((ext_vector_type(2))) float;
using u32x4 = __attribute__((ext_vector_type(4))) unsigned int;
using u32x2 = __attribute__((ext_vector_type(2))) unsigned int;

#define GROUP 16
#define CAP 128
#define CNT_STRIDE 32
#define W1_ELEMS 16384
#define W2_BYTES 98304
#define WR_BYTES 12288
#define WL_BYTES (W2_BYTES + WR_BYTES)
#define K2_WAVES 8
#define SCR_BYTES 2560

__device__ __forceinline__ float bf2f(u16 x) { return __uint_as_float(((u32)x) << 16); }
__device__ __forceinline__ u16 f2bf(float x) {
  __hip_bfloat16 b = __float2bfloat16(x);
  return *reinterpret_cast<u16*>(&b);
}
__device__ __forceinline__ float rb(float x) { return bf2f(f2bf(x)); }
typedef __bf16 hbf2 __attribute__((ext_vector_type(2)));
__device__ __forceinline__ u32 pack2(float lo, float hi) {
  f32x2 f = {lo, hi};
  hbf2 b = __builtin_convertvector(f, hbf2);
  return *reinterpret_cast<u32*>(&b);
}
__device__ __forceinline__ float lo16(u32 x) { return __uint_as_float(x << 16); }
__device__ __forceinline__ float hi16(u32 x) { return __uint_as_float(x & 0xffff0000u); }

struct WsLayout {
  size_t cnt, w1f, wl, pos4, h, rec16, rbf, ovf16, ovfg, total;
};
static inline size_t al256(size_t x) { return (x + 255) & ~(size_t)255; }
static inline WsLayout ws_layout(int N, int ngroups, int E) {
  WsLayout L;
  size_t o = 0;
  L.cnt = o;   o = al256(o + (size_t)(ngroups + 1) * CNT_STRIDE * 4);
  L.w1f = o;   o = al256(o + W1_ELEMS * 2);
  L.wl = o;    o = al256(o + WL_BYTES);
  L.pos4 = o;  o = al256(o + (size_t)N * 8);
  L.h = o;     o = al256(o + (size_t)N * 256);
  L.rec16 = o; o = al256(o + (size_t)ngroups * CAP * 16);
  L.rbf = o;   o = al256(o + ((size_t)ngroups * CAP + (size_t)E) * 32);
  L.ovf16 = o; o = al256(o + (size_t)E * 16);
  L.ovfg = o;  o = al256(o + (size_t)E * 4);
  L.total = o;
  return L;
}

__global__ __launch_bounds__(256) void k_prep(const u16* __restrict__ w1, const u16* __restrict__ w2,
                                              const u16* __restrict__ wr, int* __restrict__ cnt,
                                              u16* __restrict__ w1f, u16* __restrict__ wl,
                                              const u16* __restrict__ pos, u32x2* __restrict__ pos4,
                                              int N, int ngroups) {
  const int idx = (int)blockIdx.x * 256 + threadIdx.x;
  if (idx < 16384) {
    int j = idx & 7, lane = (idx >> 3) & 63, ks = (idx >> 9) & 3, t = idx >> 11;
    int q = lane >> 4, n = lane & 15;
    w1f[idx] = w1[(32 * ks + 8 * q + j) * 128 + 16 * t + n];
  } else if (idx < 16384 + 49152) {
    int i = idx - 16384;
    int j = i & 7, lane = (i >> 3) & 63, ks = (i >> 9) & 3, st = i >> 11;
    int hf = st / 12, tp = (st % 12) / 3, c = st % 3;
    int q = lane >> 4, n = lane & 15;
    int u = 16 * (2 * ks + (j >> 2)) + 4 * q + (j & 3);
    wl[i] = w2[u * 384 + 128 * c + 64 * hf + 4 * n + tp];
  } else if (idx < 16384 + 49152 + 6144) {
    int i = idx - 16384 - 49152;
    int j = i & 3, lane = (i >> 2) & 63, st = i >> 8;
    int hf = st / 12, tp = (st % 12) / 3, c = st % 3;
    int q = lane >> 4, n = lane & 15;
    wl[49152 + i] = wr[(4 * q + j) * 384 + 128 * c + 64 * hf + 4 * n + tp];
  }
  if (idx <= ngroups) cnt[idx * CNT_STRIDE] = 0;
  if (idx < N) {
    u32x2 p;
    p[0] = (u32)pos[idx * 3] | ((u32)pos[idx * 3 + 1] << 16);
    p[1] = (u32)pos[idx * 3 + 2];
    pos4[idx] = p;
  }
}

__global__ __launch_bounds__(256) void k_edges(
    const u16* __restrict__ sv, const u32x2* __restrict__ pos4, const int* __restrict__ src,
    const int* __restrict__ dst, const u16* __restrict__ w1f, int* __restrict__ cnt,
    u16* __restrict__ hbuf, const u16* __restrict__ freqs, int4* __restrict__ rec16,
    u32x4* __restrict__ rbfo, int4* __restrict__ ovf16, int* __restrict__ ovfg, int N, int E,
    int ngroups, int h_blocks) {
  __shared__ __attribute__((aligned(16))) u32x4 w1s[2048];
  if ((int)blockIdx.x < h_blocks) {
    const int lane = threadIdx.x & 63, wave = threadIdx.x >> 6;
    const int q = lane >> 4, n = lane & 15;
    const int node0 = ((int)blockIdx.x * 4 + wave) * 32;
    {
      const u32x4* g4 = reinterpret_cast<const u32x4*>(w1f);
      u32x4 tmp[8];
#pragma unroll
      for (int k = 0; k < 8; ++k) tmp[k] = g4[threadIdx.x + k * 256];
      bf16x8 sB[2][4];
#pragma unroll
      for (int u = 0; u < 2; ++u) {
        int node = node0 + 16 * u + n;
        if (node >= N) node = N - 1;
        const bf16x8* srow = reinterpret_cast<const bf16x8*>(sv + (size_t)node * 512);
#pragma unroll
        for (int ks = 0; ks < 4; ++ks) sB[u][ks] = srow[4 * ks + q];
      }
      __builtin_amdgcn_sched_barrier(0);
#pragma unroll
      for (int k = 0; k < 8; ++k) w1s[threadIdx.x + k * 256] = tmp[k];
      __syncthreads();
      const bf16x8* W1f = reinterpret_cast<const bf16x8*>(w1s);
      u32x4 hp[2][4];
#pragma unroll
      for (int t = 0; t < 8; ++t) {
        f32x4 d1[2] = {{0.f, 0.f, 0.f, 0.f}, {0.f, 0.f, 0.f, 0.f}};
#pragma unroll
        for (int ks = 0; ks < 4; ++ks) {
          const bf16x8 wfr = W1f[(t * 4 + ks) * 64 + lane];
          d1[0] = __builtin_amdgcn_mfma_f32_16x16x32_bf16(wfr, sB[0][ks], d1[0], 0, 0, 0);
          d1[1] = __builtin_amdgcn_mfma_f32_16x16x32_bf16(wfr, sB[1][ks], d1[1], 0, 0, 0);
        }
#pragma unroll
        for (int u = 0; u < 2; ++u) {
          float sl[4];
#pragma unroll
          for (int r = 0; r < 4; ++r) {
            const float a = rb(d1[u][r]);
            sl[r] = a * __builtin_amdgcn_rcpf(1.0f + __expf(-a));
          }
          hp[u][t >> 1][(t & 1) * 2 + 0] = pack2(sl[0], sl[1]);
          hp[u][t >> 1][(t & 1) * 2 + 1] = pack2(sl[2], sl[3]);
        }
      }
#pragma unroll
      for (int u = 0; u < 2; ++u) {
        const int node = node0 + 16 * u + n;
        if (node < N) {
          u32x4* hrow = reinterpret_cast<u32x4*>(hbuf + (size_t)node * 128);
#pragma unroll
          for (int ks = 0; ks < 4; ++ks) hrow[4 * ks + q] = hp[u][ks];
        }
      }
    }
    return;
  }
  u32x4* lst = w1s;
  int* lcnt = reinterpret_cast<int*>(w1s + 1024);
  if (threadIdx.x == 0) *lcnt = 0;
  __syncthreads();
  const int e0 = ((int)blockIdx.x - h_blocks) * 512 + threadIdx.x;
  int ee[2] = {e0, e0 + 256}, ss[2], dv[2];
#pragma unroll
  for (int u = 0; u < 2; ++u) {
    const bool ok = ee[u] < E;
    ss[u] = ok ? src[ee[u]] : 0;
    dv[u] = ok ? dst[ee[u]] : 0;
  }
  u32x2 ps[2], pd[2];
#pragma unroll
  for (int u = 0; u < 2; ++u) { ps[u] = pos4[ss[u]]; pd[u] = pos4[dv[u]]; }
#pragma unroll
  for (int u = 0; u < 2; ++u) {
    const float rx = rb(lo16(ps[u][0]) - lo16(pd[u][0]));
    const float ry = rb(hi16(ps[u][0]) - hi16(pd[u][0]));
    const float rz = rb(lo16(ps[u][1]) - lo16(pd[u][1]));
    float q2 = rb(rb(rx * rx) + rb(ry * ry) + rb(rz * rz));
    q2 = rb(q2 + 1e-8f);
    const float dd = rb(sqrtf(q2));
    if (ee[u] < E && dd < 5.0f) {
      const int p = atomicAdd(lcnt, 1);
      u32x4 a;
      a[0] = (u32)ee[u]; a[1] = (u32)ss[u]; a[2] = (u32)dv[u];
      a[3] = pack2(rx, ry);
      lst[2 * p] = a;
      lst[2 * p + 1][0] = pack2(rz, dd);
    }
  }
  __syncthreads();
  const int nact = *lcnt;
  for (int p = threadIdx.x; p < nact; p += 256) {
    const u32x4 a = lst[2 * p];
    const u32 zd = lst[2 * p + 1][0];
    const int e = (int)a[0], s = (int)a[1], d = (int)a[2];
    const float rx = lo16(a[3]), ry = hi16(a[3]), rz = lo16(zd), dd = hi16(zd);
    const int g = d >> 4;
    const int slot = atomicAdd(&cnt[g * CNT_STRIDE], 1);
    const float hx = rb(rx / dd), hy = rb(ry / dd), hz = rb(rz / dd);
    const float fc = rb(0.5f * rb(rb(cosf(rb(rb(3.141592653589793f * dd) / 5.0f))) + 1.0f));
    int4 r;
    r.x = (e << 4) | (d & 15);
    r.y = s;
    r.z = (int)pack2(hx, hy);
    r.w = (int)pack2(hz, fc);
    u32x4 rq[2];
#pragma unroll
    for (int k = 0; k < 8; ++k) {
      const u32 fq = *reinterpret_cast<const u32*>(freqs + 2 * k);
      const float s0 = rb(__sinf(rb(dd * lo16(fq))));
      const float s1 = rb(__sinf(rb(dd * hi16(fq))));
      rq[k >> 2][k & 3] = pack2(s0 / dd, s1 / dd);
    }
    size_t ridx;
    if (slot < CAP) {
      ridx = (size_t)g * CAP + slot;
      rec16[ridx] = r;
    } else {
      const int o = atomicAdd(&cnt[ngroups * CNT_STRIDE], 1);
      ovf16[o] = r;
      ovfg[o] = g;
      ridx = (size_t)ngroups * CAP + o;
    }
    rbfo[ridx * 2] = rq[0];
    rbfo[ridx * 2 + 1] = rq[1];
  }
}

__device__ __forceinline__ float bfx(const bf16x8& v, int i) {
  return __uint_as_float(((u32)(u16)v[i]) << 16);
}
__device__ __forceinline__ bf16x4 pack4(float a, float b, float c, float d) {
  u32x2 p;
  p[0] = pack2(a, b);
  p[1] = pack2(c, d);
  return *reinterpret_cast<bf16x4*>(&p);
}

__global__ __launch_bounds__(512) void k_message(
    const u16* __restrict__ sv, const int* __restrict__ cnt, const u16* __restrict__ wl_g,
    const u16* __restrict__ hbuf, const int4* __restrict__ rec16, const u16* __restrict__ rbfa,
    const int4* __restrict__ ovf16, const int* __restrict__ ovfg, u16* __restrict__ out, int N,
    int ngroups) {
  extern __shared__ __attribute__((aligned(16))) char smem[];
  const int tid = threadIdx.x;
  const int lane = tid & 63, wave = tid >> 6;

  {
    const u32x4* g4 = reinterpret_cast<const u32x4*>(wl_g);
    u32x4* l4 = reinterpret_cast<u32x4*>(smem);
    constexpr int NST = WL_BYTES / 16;
    constexpr int NIT = (NST + 511) / 512;
    u32x4 tmp[NIT];
#pragma unroll
    for (int k = 0; k < NIT; ++k) {
      const int i = tid + k * 512;
      if (i < NST) tmp[k] = g4[i];
    }
    __builtin_amdgcn_sched_barrier(0);
#pragma unroll
    for (int k = 0; k < NIT; ++k) {
      const int i = tid + k * 512;
      if (i < NST) l4[i] = tmp[k];
    }
  }
  const bf16x8* W2f = reinterpret_cast<const bf16x8*>(smem);
  const bf16x4* Wrf = reinterpret_cast<const bf16x4*>(smem + W2_BYTES);
  int* scr = reinterpret_cast<int*>(smem + WL_BYTES + wave * SCR_BYTES);
  int* s_key = scr;
  int* s_src = scr + 128;
  int* s_hxy = scr + 256;
  int* s_hzf = scr + 384;
  int* s_ri = scr + 512;

  __syncthreads();

  const char* svb = reinterpret_cast<const char*>(sv);
  const char* hb = reinterpret_cast<const char*>(hbuf);
  const char* rbb = reinterpret_cast<const char*>(rbfa);

  for (int k = wave; (int)blockIdx.x + (int)gridDim.x * (k >> 1) < ngroups; k += K2_WAVES) {
    const int g = (int)blockIdx.x + (int)gridDim.x * (k >> 1);
    const int hf = k & 1;
    const int total = cnt[g * CNT_STRIDE];
    const int nb = total < CAP ? total : CAP;
    const int ovf_n = (total > CAP) ? cnt[ngroups * CNT_STRIDE] : 0;

    u32x2 own[4][4];
    {
      int ol = lane;
      asm volatile("" : "+v"(ol));
#pragma unroll
      for (int r = 0; r < 4; ++r) {
        int node = g * GROUP + 4 * (ol >> 4) + r;
        if (node >= N) node = N - 1;
        const u32 o = (u32)node * 1024u + 128u * (u32)hf + 8u * (u32)(ol & 15);
#pragma unroll
        for (int c = 0; c < 4; ++c) own[c][r] = *reinterpret_cast<const u32x2*>(svb + (o + 256u * c));
      }
    }
    {
      int bl = lane;
      asm volatile("" : "+v"(bl));
      int4 r0 = rec16[(size_t)g * CAP + bl], r1 = rec16[(size_t)g * CAP + bl + 64];
      if (bl >= nb) r0.x = 0x7fffffff;
      if (bl + 64 >= nb) r1.x = 0x7fffffff;
      s_key[bl] = r0.x;
      s_key[bl + 64] = r1.x;
      __builtin_amdgcn_wave_barrier();
      int k0 = 0, k1 = 64;
      for (int i = 0; i < nb; i += 4) {
        int4 kk = *reinterpret_cast<const int4*>(s_key + i);
        k0 += (kk.x < r0.x) + (kk.y < r0.x) + (kk.z < r0.x) + (kk.w < r0.x);
      }
      if (nb > 64) {
        k1 = 0;
        for (int i = 0; i < nb; i += 4) {
          int4 kk = *reinterpret_cast<const int4*>(s_key + i);
          k1 += (kk.x < r1.x) + (kk.y < r1.x) + (kk.z < r1.x) + (kk.w < r1.x);
        }
      }
      if (bl >= nb) { k0 = bl; r0 = make_int4(-1, 0, 0, 0); }
      if (bl + 64 >= nb) { k1 = bl + 64; r1 = make_int4(-1, 0, 0, 0); }
      __builtin_amdgcn_wave_barrier();
      s_key[k0] = r0.x; s_src[k0] = r0.y; s_hxy[k0] = r0.z; s_hzf[k0] = r0.w; s_ri[k0] = g * CAP + (bl < nb ? bl : 0);
      s_key[k1] = r1.x; s_src[k1] = r1.y; s_hxy[k1] = r1.z; s_hzf[k1] = r1.w; s_ri[k1] = g * CAP + (bl + 64 < nb ? bl + 64 : 0);
      __builtin_amdgcn_wave_barrier();
    }

    f32x4 acc[4][4];

#pragma unroll
    for (int c = 0; c < 4; ++c)
#pragma unroll
      for (int r = 0; r < 4; ++r) {
        acc[c][0][r] = lo16(own[c][r][0]); acc[c][1][r] = hi16(own[c][r][0]);
        acc[c][2][r] = lo16(own[c][r][1]); acc[c][3][r] = hi16(own[c][r][1]);
      }
    int have = nb;
    int ovf_pos = 0;

    struct Tile {
      bf16x8 hA[4];
      u32x2 vr[4][3];
      bf16x4 rbfA, selA;
      int4 hxy4;
      u32 hz01, hz23;
    };
    auto fetch = [&](int base, Tile& T) {
      int fl = lane;
      asm volatile("" : "+v"(fl));
      const int q_ = fl >> 4, n_ = fl & 15;
      const u32 src_e = (u32)s_src[base + n_];
      const u32 ri_e = (u32)s_ri[base + n_];
      const int4 src4 = *reinterpret_cast<const int4*>(s_src + base + 4 * q_);
      const int4 key4 = *reinterpret_cast<const int4*>(s_key + base + 4 * q_);
      T.hxy4 = *reinterpret_cast<const int4*>(s_hxy + base + 4 * q_);
      const int4 hzf4 = *reinterpret_cast<const int4*>(s_hzf + base + 4 * q_);
      {
        const int kk[4] = {key4.x, key4.y, key4.z, key4.w};
        const u32 zz[4] = {(u32)hzf4.x, (u32)hzf4.y, (u32)hzf4.z, (u32)hzf4.w};
#pragma unroll
        for (int j = 0; j < 4; ++j)
          T.selA[j] = (kk[j] >= 0 && (kk[j] & 15) == n_) ? (short)(zz[j] >> 16) : (short)0;
        T.hz01 = (zz[0] & 0xffffu) | (zz[1] << 16);
        T.hz23 = (zz[2] & 0xffffu) | (zz[3] << 16);
      }
      const u32 ho = src_e * 256u + (u32)q_ * 16u;
#pragma unroll
      for (int ks = 0; ks < 4; ++ks) T.hA[ks] = *reinterpret_cast<const bf16x8*>(hb + (ho + 64u * ks));
      T.rbfA = *reinterpret_cast<const bf16x4*>(rbb + ((size_t)ri_e * 32u + (u32)q_ * 8u));
      const u32 vo = 256u + 128u * (u32)hf + 8u * (u32)n_;
      const u32 s4[4] = {(u32)src4.x, (u32)src4.y, (u32)src4.z, (u32)src4.w};
#pragma unroll
      for (int r = 0; r < 4; ++r) {
        const u32 o = s4[r] * 1024u + vo;
#pragma unroll
        for (int x = 0; x < 3; ++x) T.vr[r][x] = *reinterpret_cast<const u32x2*>(svb + (o + 256u * x));
      }
    };
    auto compute = [&](const Tile& T) {
      int wl = lane;
      asm volatile("" : "+v"(wl));
      const bf16x8* W2h = W2f + (size_t)hf * 12 * 4 * 64 + wl;
      const bf16x4* Wrh = Wrf + (size_t)hf * 12 * 64 + wl;
      bf16x8 wb[2][4];
      bf16x4 wrb[2];
#pragma unroll
      for (int ks = 0; ks < 4; ++ks) wb[0][ks] = W2h[ks * 64];
      wrb[0] = Wrh[0];
      const u32 hxy4[4] = {(u32)T.hxy4.x, (u32)T.hxy4.y, (u32)T.hxy4.z, (u32)T.hxy4.w};
      const bf16x4 selA = T.selA;
      float hat[3][4];
#pragma unroll
      for (int r = 0; r < 4; ++r) { hat[0][r] = lo16(hxy4[r]); hat[1][r] = hi16(hxy4[r]); }
      hat[2][0] = lo16(T.hz01); hat[2][1] = hi16(T.hz01);
      hat[2][2] = lo16(T.hz23); hat[2][3] = hi16(T.hz23);
      __builtin_amdgcn_sched_barrier(0);
      f32x4 m[3];
      f32x4 d2p, wfp;
#pragma unroll
      for (int st = 0; st <= 12; ++st) {
        const int cur = st & 1;
        if (st + 1 < 12) {
#pragma unroll
          for (int ks = 0; ks < 4; ++ks) wb[cur ^ 1][ks] = W2h[((st + 1) * 4 + ks) * 64];
          wrb[cur ^ 1] = Wrh[(st + 1) * 64];
        }
        __builtin_amdgcn_sched_barrier(0);
        f32x4 d2 = {0.f, 0.f, 0.f, 0.f};
        f32x4 wf = {0.f, 0.f, 0.f, 0.f};
        if (st < 12) {
          wf = __builtin_amdgcn_mfma_f32_16x16x16bf16_1k(T.rbfA, wrb[cur], wf, 0, 0, 0);
#pragma unroll
          for (int ks = 0; ks < 4; ++ks)
            d2 = __builtin_amdgcn_mfma_f32_16x16x32_bf16(T.hA[ks], wb[cur][ks], d2, 0, 0, 0);
        }
        __builtin_amdgcn_sched_barrier(0);
        if (st > 0) {
          const int pt = (st - 1) / 3, pc = (st - 1) % 3;
#pragma unroll
          for (int r = 0; r < 4; ++r) m[pc][r] = d2p[r] * wfp[r];
          if (pc == 2) {
            acc[0][pt] = __builtin_amdgcn_mfma_f32_16x16x16bf16_1k(
                selA, pack4(m[0][0], m[0][1], m[0][2], m[0][3]), acc[0][pt], 0, 0, 0);
#pragma unroll
            for (int x = 0; x < 3; ++x) {
              float mv[4];
#pragma unroll
              for (int r = 0; r < 4; ++r) {
                const u32 w = T.vr[r][x][pt >> 1];
                const float vv = (pt & 1) ? hi16(w) : lo16(w);
                mv[r] = m[1][r] * vv + m[2][r] * hat[x][r];
              }
              acc[1 + x][pt] = __builtin_amdgcn_mfma_f32_16x16x16bf16_1k(
                  selA, pack4(mv[0], mv[1], mv[2], mv[3]), acc[1 + x][pt], 0, 0, 0);
            }
          }
        }
        d2p = d2; wfp = wf;
        __builtin_amdgcn_sched_barrier(0);
      }
    };

    while (true) {
      Tile TA, TB;
      if (have > 0) fetch(0, TA);
      for (int base = 0; base < have; base += 32) {
        if (base + 16 < have) fetch(base + 16, TB);
        __builtin_amdgcn_sched_barrier(0);
        compute(TA);
        if (base + 16 >= have) break;
        if (base + 32 < have) fetch(base + 32, TA);
        __builtin_amdgcn_sched_barrier(0);
        compute(TB);
      }
      if (ovf_pos >= ovf_n) break;
      __builtin_amdgcn_wave_barrier();
      have = 0;
      while (ovf_pos < ovf_n && have <= CAP - 64 - 16) {
        const int i = ovf_pos + lane;
        const bool mine = (i < ovf_n) && (ovfg[i] == g);
        const unsigned long long bal = __ballot(mine);
        const int before = __popcll(bal & ((1ull << lane) - 1ull));
        if (mine) {
          const int4 r = ovf16[i];
          const int p = have + before;
          s_key[p] = r.x; s_src[p] = r.y; s_hxy[p] = r.z; s_hzf[p] = r.w; s_ri[p] = ngroups * CAP + i;
        }
        have += __popcll(bal);
        ovf_pos += 64;
      }
      if (lane < 16) {
        const int p = have + lane;
        s_key[p] = -1; s_src[p] = 0; s_hxy[p] = 0; s_hzf[p] = 0; s_ri[p] = 0;
      }
      __builtin_amdgcn_wave_barrier();
    }

    int ol = lane;
    asm volatile("" : "+v"(ol));
#pragma unroll
    for (int r = 0; r < 4; ++r) {
      const int node = g * GROUP + 4 * (ol >> 4) + r;
      if (node < N) {
#pragma unroll
        for (int c = 0; c < 4; ++c) {
          const size_t off = (size_t)node * 512 + 128 * c + 64 * hf + 4 * (ol & 15);
          u32x2 o;
          o[0] = pack2(acc[c][0][r], acc[c][1][r]);
          o[1] = pack2(acc[c][2][r], acc[c][3][r]);
          *reinterpret_cast<u32x2*>(out + off) = o;
        }
      }
    }
    __builtin_amdgcn_wave_barrier();
  }
}

extern "C" void kernel_launch(void* const* d_in, const int* in_sizes, int n_in,
                              void* d_out, int out_size, void* d_ws, size_t ws_size,
                              hipStream_t stream) {
  const u16* sv = (const u16*)d_in[0];
  const u16* pos = (const u16*)d_in[1];
  const int* src = (const int*)d_in[2];
  const int* dst = (const int*)d_in[3];
  const u16* freqs = (const u16*)d_in[4];
  const u16* w1 = (const u16*)d_in[5];
  const u16* w2 = (const u16*)d_in[6];
  const u16* wr = (const u16*)d_in[7];
  const int N = in_sizes[0] / 512;
  const int E = in_sizes[2];
  const int ngroups = (N + GROUP - 1) / GROUP;
  const WsLayout L = ws_layout(N, ngroups, E);
  char* ws = (char*)d_ws;
  int* cnt = (int*)(ws + L.cnt);
  u16* w1f = (u16*)(ws + L.w1f);
  u16* wl = (u16*)(ws + L.wl);
  u32x2* pos4 = (u32x2*)(ws + L.pos4);
  u16* hbuf = (u16*)(ws + L.h);
  int4* rec16 = (int4*)(ws + L.rec16);
  u16* rbfa = (u16*)(ws + L.rbf);
  int4* ovf16 = (int4*)(ws + L.ovf16);
  int* ovfg = (int*)(ws + L.ovfg);

  int prep_n = (71680 > ngroups + 1) ? 71680 : ngroups + 1;
  if (N > prep_n) prep_n = N;
  k_prep<<<(prep_n + 255) / 256, 256, 0, stream>>>(w1, w2, wr, cnt, w1f, wl, pos, pos4, N, ngroups);
  const int h_blocks = (N + 127) / 128;
  const int edge_blocks = (E + 511) / 512;
  k_edges<<<h_blocks + edge_blocks, 256, 0, stream>>>(sv, pos4, src, dst, w1f, cnt, hbuf, freqs, rec16,
                                                       (u32x4*)rbfa, ovf16, ovfg, N, E, ngroups, h_blocks);
  const size_t lds = WL_BYTES + K2_WAVES * SCR_BYTES;
  static_assert(WL_BYTES + K2_WAVES * SCR_BYTES <= 163840, "lds");
  int grid = 256;
  if (ngroups < grid) grid = ngroups;
  k_message<<<grid, 512, lds, stream>>>(sv, cnt, wl, hbuf, rec16, rbfa, ovf16, ovfg, (u16*)d_out, N,
                                        ngroups);
}
```
